```python
import torch

def _softmax(x, axis=-1):
    return torch.softmax(x, dim=axis)
def _gelu(x):
    return torch.nn.functional.gelu(x, approximate="tanh")
def _silu(x):
    return torch.nn.functional.silu(x)
def _sigmoid(x):
    return torch.sigmoid(x)
def _layernorm(x, axis=-1, eps=1e-5):
    m = x.mean(dim=axis, keepdim=True)
    v = x.var(dim=axis, keepdim=True, unbiased=False)
    return (x - m) / torch.sqrt(v + eps)
def _scatter_add(tgt, idx, src, axis=0):
    return tgt.clone().index_add_(axis, idx, src)
def _segment_sum(src, seg, n):
    out = torch.zeros((n,) + src.shape[1:], dtype=src.dtype)
    return out.index_add_(0, seg, src)
def _pad(x, pw):
    flat = tuple(v for pair in reversed(pw) for v in pair)
    return torch.nn.functional.pad(x, flat)

kernel_name = 'standalone__paged_block_table_kv_pagedattention__prompt'

def setup_inputs():

    _w = (lambda a: (lambda t: t.to(torch.bfloat16) if t.is_floating_point() else t)(torch.as_tensor(a)))
    B, Hq, Hkv, Sq, Skv, D = 1, 16, 8, 2048, 2048, 256
    bs = 16
    nbps = max(1, Skv // bs)
    num_phys = B * nbps + 4
    bt = torch.randperm(num_phys)[: B * nbps].reshape(B, nbps).to(torch.int64)
    cl = (nbps * bs - (torch.arange(1, B + 1, dtype=torch.int64) * 2654435761) % max(1, nbps * bs // 2)).to(torch.int64)
    return {
        "q": _w((lambda *s: torch.randn(*s, dtype=torch.bfloat16) * 0.8)(B, Hq, Sq, D)),
        "k_cache": _w((lambda *s: torch.randn(*s, dtype=torch.bfloat16) * 0.8)(num_phys, bs, Hkv, D)),
        "v_cache": _w((lambda *s: torch.randn(*s, dtype=torch.bfloat16) * 0.05)(num_phys, bs, Hkv, D)),
        "block_table": _w(bt),
        "context_lens": _w(cl),
    }

def reference(q, k_cache, v_cache, block_table, context_lens):
    B, Hq, Sq, D = q.shape
    bs = k_cache.shape[1]
    Hkv = k_cache.shape[2]
    nbps = block_table.shape[1]
    Skv = nbps * bs
    bt = block_table.reshape(-1)
    k = (lambda x, *d: x.permute(*d))(k_cache[bt].reshape(B, Skv, Hkv, D), 0, 2, 1, 3)
    v = (lambda x, *d: x.permute(*d))(v_cache[bt].reshape(B, Skv, Hkv, D), 0, 2, 1, 3)
    g = Hq // Hkv
    qg = q.reshape(B, Hkv, g, Sq, D)
    scores = torch.einsum("bhgqd,bhkd->bhgqk", qg, k) * (D ** -0.5)
    pos = torch.arange(Skv)
    msk = pos[None, :] < context_lens[:, None]
    scores = torch.where(msk[:, None, None, None, :], scores, -1e9)
    p = _softmax(scores, axis=-1)
    out = torch.einsum("bhgqk,bhkd->bhgqd", p, v)
    return out.reshape(B, Hq, Sq, D)
```

```cpp
#include <hip/hip_runtime.h>
#include <hip/hip_bf16.h>
#include <stdint.h>

typedef float    f32x16 __attribute__((ext_vector_type(16)));
typedef float    f32x2  __attribute__((ext_vector_type(2)));
typedef __bf16   bf16x8 __attribute__((ext_vector_type(8)));
typedef __bf16   bf16x4 __attribute__((ext_vector_type(4)));
typedef __bf16   bf16x2 __attribute__((ext_vector_type(2)));
typedef short    s16x4  __attribute__((ext_vector_type(4)));
typedef unsigned u32x4  __attribute__((ext_vector_type(4)));
typedef unsigned u32x2  __attribute__((ext_vector_type(2)));

constexpr int HQ = 16, HKV = 8, SQ = 2048, D = 256, BS = 16, NBPS = 128, SKV = 2048;
constexpr int KT = 32;

__device__ __forceinline__ unsigned cvtpk(float lo, float hi) {
  f32x2 v = {lo, hi}; bf16x2 b = __builtin_convertvector(v, bf16x2); return __builtin_bit_cast(unsigned, b);
}

__global__ __launch_bounds__(256, 1) void attn_v0(const uint16_t* __restrict__ q, const uint16_t* __restrict__ kc,
                                               const uint16_t* __restrict__ vc, const int* __restrict__ bt,
                                               const int* __restrict__ cl, uint16_t* __restrict__ out) {
  __shared__ __attribute__((aligned(16))) char lds[2 * KT * D * 2];
  char* Ks = lds; char* Vs = lds + KT * D * 2;
  const int tid = threadIdx.x, lane = tid & 63, wid = tid >> 6, r = lane & 31, h = lane >> 5;
  const int blk = blockIdx.x;
  const int kvh = blk & 7;
  const int rem = blk >> 3;
  const int qh = kvh * 2 + (rem >> 4);
  const int q0 = (rem & 15) * 128 + wid * 32;
  const int ctx = cl[0];
  int nkeys = ctx > 0 ? (ctx < SKV ? ctx : SKV) : SKV;
  const int ntiles = (nkeys + KT - 1) / KT;
  bf16x8 qf[16];
  const uint16_t* qrow = q + ((size_t)qh * SQ + q0 + r) * D;
#pragma unroll
  for (int s = 0; s < 16; ++s) qf[s] = *(const bf16x8*)(qrow + 16 * s + 8 * h);
  f32x16 o[8];
#pragma unroll
  for (int i = 0; i < 8; ++i) o[i] = f32x16{};
  float m = -INFINITY, l = 0.f;
  const float C = 1.4426950408889634f / 16.f;
  for (int t = 0; t < ntiles; ++t) {
    __syncthreads();
#pragma unroll
    for (int i = 0; i < 4; ++i) {
      const int n = tid + 256 * i, row = n >> 5, ch = n & 31;
      const int key = t * KT + row;
      const int page = bt[key >> 4];
      const size_t g = (((size_t)page * BS + (key & 15)) * HKV + kvh) * D + ch * 8;
      u32x4 kv = *(const u32x4*)(kc + g);
      u32x4 vv = *(const u32x4*)(vc + g);
      *(u32x4*)(Ks + row * 512 + ((ch ^ (row & 15)) * 16)) = kv;
      *(u32x4*)(Vs + ((row >> 3) * 8 + (ch >> 2)) * 512 + (row & 7) * 64 + (ch & 3) * 16) = vv;
    }
    __syncthreads();
    f32x16 s = f32x16{};
#pragma unroll
    for (int ks = 0; ks < 16; ++ks) {
      const int ch = 2 * ks + h;
      bf16x8 kf = *(const bf16x8*)(Ks + r * 512 + ((ch ^ (r & 15)) * 16));
      s = __builtin_amdgcn_mfma_f32_32x32x16_bf16(kf, qf[ks], s, 0, 0, 0);
    }
    float x[16];
    float mx = -INFINITY;
#pragma unroll
    for (int i = 0; i < 16; ++i) {
      const int key = t * KT + (i & 3) + 8 * (i >> 2) + 4 * h;
      float v = s[i] * C;
      if (key >= ctx) v = -1e9f * 1.4426950408889634f;
      x[i] = v; mx = fmaxf(mx, v);
    }
    mx = fmaxf(mx, __shfl_xor(mx, 32));
    const float mn = fmaxf(m, mx);
    const float alpha = exp2f(m - mn);
    m = mn;
    float ps = 0.f;
#pragma unroll
    for (int i = 0; i < 16; ++i) { x[i] = exp2f(x[i] - mn); ps += x[i]; }
    l = l * alpha + ps;
#pragma unroll
    for (int d = 0; d < 8; ++d)
#pragma unroll
      for (int i = 0; i < 16; ++i) o[d][i] *= alpha;
    bf16x8 pf[2];
#pragma unroll
    for (int ks = 0; ks < 2; ++ks) {
      u32x4 w;
#pragma unroll
      for (int j = 0; j < 4; ++j) w[j] = cvtpk(x[8 * ks + 2 * j], x[8 * ks + 2 * j + 1]);
      pf[ks] = __builtin_bit_cast(bf16x8, w);
    }
    const unsigned vbase = (unsigned)(size_t)Vs + (4 * h + ((lane & 15) >> 2)) * 64 + ((lane >> 4) & 1) * 32 + (lane & 3) * 8;
#pragma unroll
    for (int d = 0; d < 8; ++d) {
#pragma unroll
      for (int ks = 0; ks < 2; ++ks) {
        s16x4 lo, hi;
        asm volatile("ds_read_b64_tr_b16 %0, %2 offset:%3\n\tds_read_b64_tr_b16 %1, %2 offset:%4\n\ts_waitcnt lgkmcnt(0)"
                     : "=&v"(lo), "=&v"(hi) : "v"(vbase), "i"((2 * ks) * 4096 + d * 512), "i"((2 * ks + 1) * 4096 + d * 512) : "memory");
        typedef short s16x8 __attribute__((ext_vector_type(8)));
        s16x8 vv = {lo[0], lo[1], lo[2], lo[3], hi[0], hi[1], hi[2], hi[3]};
        o[d] = __builtin_amdgcn_mfma_f32_32x32x16_bf16(__builtin_bit_cast(bf16x8, vv), pf[ks], o[d], 0, 0, 0);
      }
    }
  }
  l += __shfl_xor(l, 32);
  const float il = 1.f / l;
  uint16_t* orow = out + ((size_t)qh * SQ + q0 + r) * D;
#pragma unroll
  for (int d = 0; d < 8; ++d)
#pragma unroll
    for (int g = 0; g < 4; ++g) {
      u32x2 w;
      w[0] = cvtpk(o[d][4 * g] * il, o[d][4 * g + 1] * il);
      w[1] = cvtpk(o[d][4 * g + 2] * il, o[d][4 * g + 3] * il);
      *(u32x2*)(orow + 32 * d + 8 * g + 4 * h) = w;
    }
}

extern "C" void kernel_launch(void* const* d_in, const int* in_sizes, int n_in,
                              void* d_out, int out_size, void* d_ws, size_t ws_size,
                              hipStream_t stream) {
  attn_v0<<<256, 256, 0, stream>>>((const uint16_t*)d_in[0], (const uint16_t*)d_in[1], (const uint16_t*)d_in[2],
                                  (const int*)d_in[3], (const int*)d_in[4], (uint16_t*)d_out);
}
```

```cpp
#include <hip/hip_runtime.h>
#include <stdint.h>

__global__ __launch_bounds__(256, 1) void paged_attn_kernel(const void* __restrict__ q, const void* __restrict__ kc, const void* __restrict__ vc,
                                                           const int* __restrict__ bt, const int* __restrict__ cl, void* __restrict__ out,
                                                           unsigned kv_bytes) {
  __shared__ __attribute__((aligned(1024))) char lds[163840];
  const unsigned wid = __builtin_amdgcn_readfirstlane(threadIdx.x >> 6);
  asm volatile(
    "s_load_dwordx8 s[52:59], %0, 0x0\n"
    "s_load_dwordx4 s[72:75], %0, 0x20\n"
    "s_load_dword s70, %0, 0x30\n"
    "v_mbcnt_lo_u32_b32 v94, -1, 0\n"
    "v_mbcnt_hi_u32_b32 v94, -1, v94\n"
    "s_getpc_b64 s[36:37]\n"
    ".Lpc0:\n"
    "v_lshlrev_b32 v123, 7, v94\n"
    "s_add_u32 s38, s36, .Lpc0-.Lpc0\n"
    "s_addc_u32 s39, s37, 0\n"
    "s_mov_b32 s49, .Lend-.Lpc0-4\n"
    "v_min_u32 v122, s49, v123\n"
    "global_load_dword v124, v122, s[38:39]\n"
    "s_add_u32 s38, s36, .Ltop0-.Lpc0\n"
    "s_addc_u32 s39, s37, 0\n"
    "s_mov_b32 s49, .Lend-.Ltop0-4\n"
    "v_min_u32 v122, s49, v123\n"
    "global_load_dword v125, v122, s[38:39]\n"
    "s_add_u32 s38, s36, .Lpf2-.Lpc0\n"
    "s_addc_u32 s39, s37, 0\n"
    "s_mov_b32 s49, .Lend-.Lpf2-4\n"
    "v_min_u32 v122, s49, v123\n"
    "global_load_dword v126, v122, s[38:39]\n"
    "s_mov_b32 s41, %1\n"
    "s_mov_b32 s71, %2\n"
    "s_mov_b32 s69, %3\n"
    "s_and_b32 s62, s71, 7\n"
    "s_lshl_b32 s49, s62, 21\n"
    "s_lshr_b32 s63, s71, 3\n"
    "s_lshl_b32 s65, s63, 16\n"
    "s_add_u32 s49, s49, s65\n"
    "s_lshl_b32 s65, s41, 14\n"
    "s_add_u32 s66, s49, s65\n"
    "s_lshr_b32 s49, s41, 1\n"
    "s_lshl_b32 s49, s49, 2\n"
    "v_lshlrev_b32 v103, 3, v94\n"
    "v_add_u32 v103, s49, v103\n"
    "v_lshrrev_b32 v96, 4, v94\n"
    "v_lshrrev_b32 v97, 3, v94\n"
    "v_and_b32 v98, 7, v94\n"
    "v_xor_b32 v99, v98, v96\n"
    "v_lshlrev_b32 v99, 4, v99\n"
    "v_lshl_add_u32 v99, v97, 9, v99\n"
    "s_lshl_b32 s65, s41, 12\n"
    "s_add_u32 s45, s65, s69\n"
    "v_add_u32 v104, 4096, v99\n"
    "v_add_u32 v105, 3200, v99\n"
    "v_add_u32 v106, 2304, v99\n"
    "v_add_u32 v107, 1408, v99\n"
    "v_xor_b32 v108, 64, v104\n"
    "v_xor_b32 v109, 64, v105\n"
    "v_xor_b32 v110, 64, v106\n"
    "v_xor_b32 v111, 64, v107\n"
    "s_add_u32 s46, s45, 65536\n"
    "v_and_b32 v95, 15, v94\n"
    "s_and_b32 s49, s41, 1\n"
    "s_lshl_b32 s65, s49, 2\n"
    "v_add_u32 v99, s65, v96\n"
    "v_xor_b32 v99, v98, v99\n"
    "v_lshlrev_b32 v99, 4, v99\n"
    "v_lshl_add_u32 v99, v97, 12, v99\n"
    "v_add_u32 v75, 4096, v99\n"
    "v_add_u32 v76, 3200, v99\n"
    "v_add_u32 v77, 2304, v99\n"
    "v_add_u32 v78, 1408, v99\n"
    "s_waitcnt lgkmcnt(0)\n"
    "global_load_dword v83, v103, s[58:59]\n"
    "s_load_dword s40, s[72:73], 0x0\n"
    "s_add_u32 s36, s52, s66\n"
    "s_addc_u32 s37, s53, 0\n"
    "s_sub_u32 s36, s36, 0x1000\n"
    "s_subb_u32 s37, s37, 0\n"
    "s_and_b32 s37, s37, 0xffff\n"
    "s_mov_b32 s38, 0x5000\n"
    "s_mov_b32 s39, 0x20000\n"
    "s_add_u32 m0, s45, 131072\n"
    "s_mov_b32 s67, 0\n"
    "buffer_load_dwordx4 v104, s[36:39], s67 offen offset:0 nt lds\n"
    "buffer_load_dwordx4 v105, s[36:39], s67 offen offset:1024 nt lds\n"
    "buffer_load_dwordx4 v106, s[36:39], s67 offen offset:2048 nt lds\n"
    "buffer_load_dwordx4 v107, s[36:39], s67 offen offset:3072 nt lds\n"
    "s_add_u32 m0, s45, 147456\n"
    "s_mov_b32 s67, 4096\n"
    "buffer_load_dwordx4 v108, s[36:39], s67 offen offset:0 nt lds\n"
    "buffer_load_dwordx4 v109, s[36:39], s67 offen offset:1024 nt lds\n"
    "buffer_load_dwordx4 v110, s[36:39], s67 offen offset:2048 nt lds\n"
    "buffer_load_dwordx4 v111, s[36:39], s67 offen offset:3072 nt lds\n"
    "s_add_u32 m0, s45, 98304\n"
    "s_mov_b32 s67, 8192\n"
    "buffer_load_dwordx4 v104, s[36:39], s67 offen offset:0 nt lds\n"
    "buffer_load_dwordx4 v105, s[36:39], s67 offen offset:1024 nt lds\n"
    "buffer_load_dwordx4 v106, s[36:39], s67 offen offset:2048 nt lds\n"
    "buffer_load_dwordx4 v107, s[36:39], s67 offen offset:3072 nt lds\n"
    "s_add_u32 m0, s45, 114688\n"
    "s_mov_b32 s67, 12288\n"
    "buffer_load_dwordx4 v108, s[36:39], s67 offen offset:0 nt lds\n"
    "buffer_load_dwordx4 v109, s[36:39], s67 offen offset:1024 nt lds\n"
    "buffer_load_dwordx4 v110, s[36:39], s67 offen offset:2048 nt lds\n"
    "buffer_load_dwordx4 v111, s[36:39], s67 offen offset:3072 nt lds\n"
    "s_add_u32 s92, s74, s66\n"
    "s_addc_u32 s93, s75, 0\n"
    "s_sub_u32 s24, s54, 0x1000\n"
    "s_subb_u32 s25, s55, 0\n"
    "s_and_b32 s25, s25, 0xffff\n"
    "s_add_u32 s48, s70, 0x1000\n"
    "s_mov_b32 s26, s48\n"
    "s_mov_b32 s27, 0x20000\n"
    "s_sub_u32 s28, s56, 0x1000\n"
    "s_subb_u32 s29, s57, 0\n"
    "s_and_b32 s29, s29, 0xffff\n"
    "s_mov_b32 s30, s48\n"
    "s_mov_b32 s31, 0x20000\n"
    "v_lshrrev_b32 v99, 1, v95\n"
    "v_lshlrev_b32 v99, 12, v99\n"
    "v_lshl_add_u32 v99, v96, 5, v99\n"
    "v_and_b32 v100, 1, v94\n"
    "v_lshl_add_u32 v99, v100, 4, v99\n"
    "v_add_u32 v79, 4096, v99\n"
    "v_add_u32 v80, 3200, v99\n"
    "v_add_u32 v81, 2304, v99\n"
    "v_add_u32 v82, 1408, v99\n"
    "v_and_b32 v99, 7, v95\n"
    "v_lshlrev_b32 v99, 7, v99\n"
    "v_lshrrev_b32 v100, 1, v95\n"
    "v_and_b32 v100, 7, v100\n"
    "v_xor_b32 v100, v96, v100\n"
    "v_lshl_add_u32 v116, v100, 4, v99\n"
    "v_xor_b32 v117, 64, v116\n"
    "v_lshrrev_b32 v101, 3, v95\n"
    "v_lshlrev_b32 v99, 12, v101\n"
    "v_add_u32 v99, s69, v99\n"
    "v_add_u32 v69, v99, v116\n"
    "v_add_u32 v70, v99, v117\n"
    "s_mov_b32 s65, 16384\n"
    "v_mul_lo_u32 v99, v101, s65\n"
    "s_add_u32 s65, s45, 131072\n"
    "v_add_u32 v99, s65, v99\n"
    "v_add_u32 v112, v99, v116\n"
    "v_add_u32 v113, v99, v117\n"
    "s_mov_b32 s65, 16384\n"
    "v_mul_lo_u32 v99, v101, s65\n"
    "s_add_u32 s65, s45, 98304\n"
    "v_add_u32 v99, s65, v99\n"
    "v_add_u32 v114, v99, v116\n"
    "v_add_u32 v115, v99, v117\n"
    "v_lshrrev_b32 v99, 1, v96\n"
    "v_lshlrev_b32 v99, 12, v99\n"
    "v_and_b32 v100, 1, v96\n"
    "v_lshl_add_u32 v99, v100, 7, v99\n"
    "v_lshrrev_b32 v102, 2, v95\n"
    "v_lshl_add_u32 v99, v102, 5, v99\n"
    "v_and_b32 v102, 3, v94\n"
    "v_lshl_add_u32 v99, v102, 3, v99\n"
    "v_add_u32 v99, s69, v99\n"
    "v_add_u32 v73, 65536, v99\n"
    "v_lshlrev_b32 v99, 14, v101\n"
    "v_and_b32 v102, 7, v95\n"
    "v_lshl_add_u32 v99, v102, 9, v99\n"
    "v_lshl_add_u32 v99, v100, 3, v99\n"
    "v_add_u32 v99, s45, v99\n"
    "v_lshrrev_b32 v103, 1, v96\n"
    "v_add_u32 v85, 0, v103\n"
    "v_xor_b32 v85, v85, v102\n"
    "v_lshl_add_u32 v85, v85, 4, v99\n"
    "v_add_u32 v86, 2, v103\n"
    "v_xor_b32 v86, v86, v102\n"
    "v_lshl_add_u32 v86, v86, 4, v99\n"
    "v_add_u32 v87, 4, v103\n"
    "v_xor_b32 v87, v87, v102\n"
    "v_lshl_add_u32 v87, v87, 4, v99\n"
    "v_add_u32 v88, 6, v103\n"
    "v_xor_b32 v88, v88, v102\n"
    "v_lshl_add_u32 v88, v88, 4, v99\n"
    "v_xor_b32 v89, v98, v97\n"
    "v_lshlrev_b32 v89, 4, v89\n"
    "v_lshl_add_u32 v89, v97, 9, v89\n"
    "v_add_u32 v89, s45, v89\n"
    "v_lshlrev_b32 v90, 4, v98\n"
    "v_lshl_add_u32 v90, v97, 9, v90\n"
    "v_add_u32 v91, 4096, v90\n"
    "v_add_u32 v92, 8192, v90\n"
    "v_add_u32 v93, 12288, v90\n"
    "s_lshl_b32 s65, s62, 9\n"
    "s_lshl_b32 s49, s49, 15\n"
    "s_add_u32 s66, s65, s49\n"
    "v_mov_b32 v128, 0\n"
    "v_mov_b32 v129, 0\n"
    "v_mov_b32 v130, 0\n"
    "v_mov_b32 v131, 0\n"
    "v_mov_b32 v132, 0\n"
    "v_mov_b32 v133, 0\n"
    "v_mov_b32 v134, 0\n"
    "v_mov_b32 v135, 0\n"
    "v_mov_b32 v136, 0\n"
    "v_mov_b32 v137, 0\n"
    "v_mov_b32 v138, 0\n"
    "v_mov_b32 v139, 0\n"
    "v_mov_b32 v140, 0\n"
    "v_mov_b32 v141, 0\n"
    "v_mov_b32 v142, 0\n"
    "v_mov_b32 v143, 0\n"
    "v_mov_b32 v144, 0\n"
    "v_mov_b32 v145, 0\n"
    "v_mov_b32 v146, 0\n"
    "v_mov_b32 v147, 0\n"
    "v_mov_b32 v148, 0\n"
    "v_mov_b32 v149, 0\n"
    "v_mov_b32 v150, 0\n"
    "v_mov_b32 v151, 0\n"
    "v_mov_b32 v152, 0\n"
    "v_mov_b32 v153, 0\n"
    "v_mov_b32 v154, 0\n"
    "v_mov_b32 v155, 0\n"
    "v_mov_b32 v156, 0\n"
    "v_mov_b32 v157, 0\n"
    "v_mov_b32 v158, 0\n"
    "v_mov_b32 v159, 0\n"
    "v_mov_b32 v160, 0\n"
    "v_mov_b32 v161, 0\n"
    "v_mov_b32 v162, 0\n"
    "v_mov_b32 v163, 0\n"
    "v_mov_b32 v164, 0\n"
    "v_mov_b32 v165, 0\n"
    "v_mov_b32 v166, 0\n"
    "v_mov_b32 v167, 0\n"
    "v_mov_b32 v168, 0\n"
    "v_mov_b32 v169, 0\n"
    "v_mov_b32 v170, 0\n"
    "v_mov_b32 v171, 0\n"
    "v_mov_b32 v172, 0\n"
    "v_mov_b32 v173, 0\n"
    "v_mov_b32 v174, 0\n"
    "v_mov_b32 v175, 0\n"
    "v_mov_b32 v176, 0\n"
    "v_mov_b32 v177, 0\n"
    "v_mov_b32 v178, 0\n"
    "v_mov_b32 v179, 0\n"
    "v_mov_b32 v180, 0\n"
    "v_mov_b32 v181, 0\n"
    "v_mov_b32 v182, 0\n"
    "v_mov_b32 v183, 0\n"
    "v_mov_b32 v184, 0\n"
    "v_mov_b32 v185, 0\n"
    "v_mov_b32 v186, 0\n"
    "v_mov_b32 v187, 0\n"
    "v_mov_b32 v188, 0\n"
    "v_mov_b32 v189, 0\n"
    "v_mov_b32 v190, 0\n"
    "v_mov_b32 v191, 0\n"
    "v_mov_b32 v192, 0\n"
    "v_mov_b32 v193, 0\n"
    "v_mov_b32 v194, 0\n"
    "v_mov_b32 v195, 0\n"
    "v_mov_b32 v196, 0\n"
    "v_mov_b32 v197, 0\n"
    "v_mov_b32 v198, 0\n"
    "v_mov_b32 v199, 0\n"
    "v_mov_b32 v200, 0\n"
    "v_mov_b32 v201, 0\n"
    "v_mov_b32 v202, 0\n"
    "v_mov_b32 v203, 0\n"
    "v_mov_b32 v204, 0\n"
    "v_mov_b32 v205, 0\n"
    "v_mov_b32 v206, 0\n"
    "v_mov_b32 v207, 0\n"
    "v_mov_b32 v208, 0\n"
    "v_mov_b32 v209, 0\n"
    "v_mov_b32 v210, 0\n"
    "v_mov_b32 v211, 0\n"
    "v_mov_b32 v212, 0\n"
    "v_mov_b32 v213, 0\n"
    "v_mov_b32 v214, 0\n"
    "v_mov_b32 v215, 0\n"
    "v_mov_b32 v216, 0\n"
    "v_mov_b32 v217, 0\n"
    "v_mov_b32 v218, 0\n"
    "v_mov_b32 v219, 0\n"
    "v_mov_b32 v220, 0\n"
    "v_mov_b32 v221, 0\n"
    "v_mov_b32 v222, 0\n"
    "v_mov_b32 v223, 0\n"
    "v_mov_b32 v224, 0\n"
    "v_mov_b32 v225, 0\n"
    "v_mov_b32 v226, 0\n"
    "v_mov_b32 v227, 0\n"
    "v_mov_b32 v228, 0\n"
    "v_mov_b32 v229, 0\n"
    "v_mov_b32 v230, 0\n"
    "v_mov_b32 v231, 0\n"
    "v_mov_b32 v232, 0\n"
    "v_mov_b32 v233, 0\n"
    "v_mov_b32 v234, 0\n"
    "v_mov_b32 v235, 0\n"
    "v_mov_b32 v236, 0\n"
    "v_mov_b32 v237, 0\n"
    "v_mov_b32 v238, 0\n"
    "v_mov_b32 v239, 0\n"
    "v_mov_b32 v240, 0\n"
    "v_mov_b32 v241, 0\n"
    "v_mov_b32 v242, 0\n"
    "v_mov_b32 v243, 0\n"
    "v_mov_b32 v244, 0\n"
    "v_mov_b32 v245, 0\n"
    "v_mov_b32 v246, 0\n"
    "v_mov_b32 v247, 0\n"
    "v_mov_b32 v248, 0\n"
    "v_mov_b32 v249, 0\n"
    "v_mov_b32 v250, 0\n"
    "v_mov_b32 v251, 0\n"
    "v_mov_b32 v252, 0\n"
    "v_mov_b32 v253, 0\n"
    "v_mov_b32 v254, 0\n"
    "v_mov_b32 v255, 0\n"
    "v_mov_b32 v32, 0\n"
    "v_mov_b32 v33, 0\n"
    "v_mov_b32 v34, 0\n"
    "v_mov_b32 v35, 0\n"
    "v_mov_b32 v36, 0\n"
    "v_mov_b32 v37, 0\n"
    "v_mov_b32 v38, 0\n"
    "v_mov_b32 v39, 0\n"
    "v_mov_b32 v40, 0\n"
    "v_mov_b32 v41, 0\n"
    "v_mov_b32 v42, 0\n"
    "v_mov_b32 v43, 0\n"
    "v_mov_b32 v44, 0\n"
    "v_mov_b32 v45, 0\n"
    "v_mov_b32 v46, 0\n"
    "v_mov_b32 v47, 0\n"
    "v_mov_b32 v66, 0\n"
    "v_mov_b32 v67, 0\n"
    "s_waitcnt lgkmcnt(0)\n"
    "s_mov_b32 s64, 0x3db8aa3b\n"
    "s_cmp_gt_i32 s40, 0\n"
    "s_cselect_b32 s64, s64, 0\n"
    "s_cselect_b32 s40, s40, 0x800\n"
    "s_min_i32 s40, s40, 0x800\n"
    "s_add_u32 s34, s40, 31\n"
    "s_lshr_b32 s34, s34, 5\n"
    "s_sub_u32 s35, s34, 1\n"
    "s_mov_b32 s47, 0x4e800000\n"
    "s_mov_b32 s68, 0xff61b1e6\n"
    "s_mov_b32 s33, 0xff800000\n"
    "v_mov_b32 v118, s64\n"
    "v_mov_b32 v119, s64\n"
    "s_getpc_b64 s[56:57]\n"
    ".Lpc1:\n"
    "s_add_u32 s50, s56, .Lslow0-.Lpc1\n"
    "s_addc_u32 s51, s57, 0\n"
    "s_add_u32 s52, s56, .Lslow1-.Lpc1\n"
    "s_addc_u32 s53, s57, 0\n"
    "s_add_u32 s54, s56, .Lmask-.Lpc1\n"
    "s_addc_u32 s55, s57, 0\n"
    "s_cmp_eq_u32 s34, 1\n"
    "s_cbranch_scc1 .Lmask0\n"
    ".Lnomask0:\n"
    "s_waitcnt vmcnt(16)\n"
    "v_lshlrev_b32 v83, 16, v83\n"
    "v_add_u32 v83, s66, v83\n"
    "s_nop 0\n"
    "v_readlane_b32 s72, v83, 0\n"
    "v_readlane_b32 s73, v83, 1\n"
    "v_readlane_b32 s74, v83, 2\n"
    "v_readlane_b32 s75, v83, 3\n"
    "v_readlane_b32 s43, v83, 4\n"
    "s_mov_b32 s42, 4\n"
    "s_nop 1\n"
    "s_add_u32 m0, s45, 0\n"
    "s_nop 0\n"
    "buffer_load_dwordx4 v75, s[24:27], s72 offen offset:0 nt lds\n"
    "buffer_load_dwordx4 v76, s[24:27], s72 offen offset:1024 nt lds\n"
    "buffer_load_dwordx4 v77, s[24:27], s72 offen offset:2048 nt lds\n"
    "buffer_load_dwordx4 v78, s[24:27], s72 offen offset:3072 nt lds\n"
    "s_add_u32 m0, s45, 16384\n"
    "s_nop 0\n"
    "buffer_load_dwordx4 v75, s[24:27], s73 offen offset:0 nt lds\n"
    "buffer_load_dwordx4 v76, s[24:27], s73 offen offset:1024 nt lds\n"
    "buffer_load_dwordx4 v77, s[24:27], s73 offen offset:2048 nt lds\n"
    "buffer_load_dwordx4 v78, s[24:27], s73 offen offset:3072 nt lds\n"
    "s_mov_b32 s44, s75\n"
    "s_waitcnt vmcnt(16)\n"
    "ds_read_b128 v[0:3], v112 offset:0\n"
    "ds_read_b128 v[4:7], v113 offset:0\n"
    "ds_read_b128 v[8:11], v112 offset:1024\n"
    "ds_read_b128 v[12:15], v113 offset:1024\n"
    "ds_read_b128 v[16:19], v112 offset:2048\n"
    "ds_read_b128 v[20:23], v113 offset:2048\n"
    "ds_read_b128 v[24:27], v112 offset:3072\n"
    "ds_read_b128 v[28:31], v113 offset:3072\n"
    "s_waitcnt lgkmcnt(7)\n"
    "v_lshlrev_b32 v120, 16, v0\n"
    "v_and_b32 v121, 0xffff0000, v0\n"
    "v_pk_mul_f32 v[120:121], v[120:121], v[118:119]\n"
    "v_cvt_pk_bf16_f32 v120, v120, v121\n"
    "v_accvgpr_write_b32 a0, v120\n"
    "v_lshlrev_b32 v122, 16, v1\n"
    "v_and_b32 v123, 0xffff0000, v1\n"
    "v_pk_mul_f32 v[122:123], v[122:123], v[118:119]\n"
    "v_cvt_pk_bf16_f32 v122, v122, v123\n"
    "v_accvgpr_write_b32 a1, v122\n"
    "v_lshlrev_b32 v120, 16, v2\n"
    "v_and_b32 v121, 0xffff0000, v2\n"
    "v_pk_mul_f32 v[120:121], v[120:121], v[118:119]\n"
    "v_cvt_pk_bf16_f32 v120, v120, v121\n"
    "v_accvgpr_write_b32 a2, v120\n"
    "v_lshlrev_b32 v122, 16, v3\n"
    "v_and_b32 v123, 0xffff0000, v3\n"
    "v_pk_mul_f32 v[122:123], v[122:123], v[118:119]\n"
    "v_cvt_pk_bf16_f32 v122, v122, v123\n"
    "v_accvgpr_write_b32 a3, v122\n"
    "s_add_u32 m0, s46, 0\n"
    "s_nop 0\n"
    "buffer_load_dwordx4 v79, s[28:31], s72 offen offset:0 nt lds\n"
    "s_waitcnt lgkmcnt(6)\n"
    "v_lshlrev_b32 v120, 16, v4\n"
    "v_and_b32 v121, 0xffff0000, v4\n"
    "v_pk_mul_f32 v[120:121], v[120:121], v[118:119]\n"
    "v_cvt_pk_bf16_f32 v120, v120, v121\n"
    "v_accvgpr_write_b32 a4, v120\n"
    "v_lshlrev_b32 v122, 16, v5\n"
    "v_and_b32 v123, 0xffff0000, v5\n"
    "v_pk_mul_f32 v[122:123], v[122:123], v[118:119]\n"
    "v_cvt_pk_bf16_f32 v122, v122, v123\n"
    "v_accvgpr_write_b32 a5, v122\n"
    "v_lshlrev_b32 v120, 16, v6\n"
    "v_and_b32 v121, 0xffff0000, v6\n"
    "v_pk_mul_f32 v[120:121], v[120:121], v[118:119]\n"
    "v_cvt_pk_bf16_f32 v120, v120, v121\n"
    "v_accvgpr_write_b32 a6, v120\n"
    "v_lshlrev_b32 v122, 16, v7\n"
    "v_and_b32 v123, 0xffff0000, v7\n"
    "v_pk_mul_f32 v[122:123], v[122:123], v[118:119]\n"
    "v_cvt_pk_bf16_f32 v122, v122, v123\n"
    "v_accvgpr_write_b32 a7, v122\n"
    "buffer_load_dwordx4 v80, s[28:31], s72 offen offset:1024 nt lds\n"
    "s_waitcnt lgkmcnt(5)\n"
    "v_lshlrev_b32 v120, 16, v8\n"
    "v_and_b32 v121, 0xffff0000, v8\n"
    "v_pk_mul_f32 v[120:121], v[120:121], v[118:119]\n"
    "v_cvt_pk_bf16_f32 v120, v120, v121\n"
    "v_accvgpr_write_b32 a8, v120\n"
    "v_lshlrev_b32 v122, 16, v9\n"
    "v_and_b32 v123, 0xffff0000, v9\n"
    "v_pk_mul_f32 v[122:123], v[122:123], v[118:119]\n"
    "v_cvt_pk_bf16_f32 v122, v122, v123\n"
    "v_accvgpr_write_b32 a9, v122\n"
    "v_lshlrev_b32 v120, 16, v10\n"
    "v_and_b32 v121, 0xffff0000, v10\n"
    "v_pk_mul_f32 v[120:121], v[120:121], v[118:119]\n"
    "v_cvt_pk_bf16_f32 v120, v120, v121\n"
    "v_accvgpr_write_b32 a10, v120\n"
    "v_lshlrev_b32 v122, 16, v11\n"
    "v_and_b32 v123, 0xffff0000, v11\n"
    "v_pk_mul_f32 v[122:123], v[122:123], v[118:119]\n"
    "v_cvt_pk_bf16_f32 v122, v122, v123\n"
    "v_accvgpr_write_b32 a11, v122\n"
    "buffer_load_dwordx4 v81, s[28:31], s72 offen offset:2048 nt lds\n"
    "s_waitcnt lgkmcnt(4)\n"
    "v_lshlrev_b32 v120, 16, v12\n"
    "v_and_b32 v121, 0xffff0000, v12\n"
    "v_pk_mul_f32 v[120:121], v[120:121], v[118:119]\n"
    "v_cvt_pk_bf16_f32 v120, v120, v121\n"
    "v_accvgpr_write_b32 a12, v120\n"
    "v_lshlrev_b32 v122, 16, v13\n"
    "v_and_b32 v123, 0xffff0000, v13\n"
    "v_pk_mul_f32 v[122:123], v[122:123], v[118:119]\n"
    "v_cvt_pk_bf16_f32 v122, v122, v123\n"
    "v_accvgpr_write_b32 a13, v122\n"
    "v_lshlrev_b32 v120, 16, v14\n"
    "v_and_b32 v121, 0xffff0000, v14\n"
    "v_pk_mul_f32 v[120:121], v[120:121], v[118:119]\n"
    "v_cvt_pk_bf16_f32 v120, v120, v121\n"
    "v_accvgpr_write_b32 a14, v120\n"
    "v_lshlrev_b32 v122, 16, v15\n"
    "v_and_b32 v123, 0xffff0000, v15\n"
    "v_pk_mul_f32 v[122:123], v[122:123], v[118:119]\n"
    "v_cvt_pk_bf16_f32 v122, v122, v123\n"
    "v_accvgpr_write_b32 a15, v122\n"
    "buffer_load_dwordx4 v82, s[28:31], s72 offen offset:3072 nt lds\n"
    "s_waitcnt lgkmcnt(3)\n"
    "v_lshlrev_b32 v120, 16, v16\n"
    "v_and_b32 v121, 0xffff0000, v16\n"
    "v_pk_mul_f32 v[120:121], v[120:121], v[118:119]\n"
    "v_cvt_pk_bf16_f32 v120, v120, v121\n"
    "v_accvgpr_write_b32 a16, v120\n"
    "v_lshlrev_b32 v122, 16, v17\n"
    "v_and_b32 v123, 0xffff0000, v17\n"
    "v_pk_mul_f32 v[122:123], v[122:123], v[118:119]\n"
    "v_cvt_pk_bf16_f32 v122, v122, v123\n"
    "v_accvgpr_write_b32 a17, v122\n"
    "v_lshlrev_b32 v120, 16, v18\n"
    "v_and_b32 v121, 0xffff0000, v18\n"
    "v_pk_mul_f32 v[120:121], v[120:121], v[118:119]\n"
    "v_cvt_pk_bf16_f32 v120, v120, v121\n"
    "v_accvgpr_write_b32 a18, v120\n"
    "v_lshlrev_b32 v122, 16, v19\n"
    "v_and_b32 v123, 0xffff0000, v19\n"
    "v_pk_mul_f32 v[122:123], v[122:123], v[118:119]\n"
    "v_cvt_pk_bf16_f32 v122, v122, v123\n"
    "v_accvgpr_write_b32 a19, v122\n"
    "s_add_u32 m0, s45, 32768\n"
    "s_nop 0\n"
    "buffer_load_dwordx4 v75, s[24:27], s74 offen offset:0 nt lds\n"
    "s_waitcnt lgkmcnt(2)\n"
    "v_lshlrev_b32 v120, 16, v20\n"
    "v_and_b32 v121, 0xffff0000, v20\n"
    "v_pk_mul_f32 v[120:121], v[120:121], v[118:119]\n"
    "v_cvt_pk_bf16_f32 v120, v120, v121\n"
    "v_accvgpr_write_b32 a20, v120\n"
    "v_lshlrev_b32 v122, 16, v21\n"
    "v_and_b32 v123, 0xffff0000, v21\n"
    "v_pk_mul_f32 v[122:123], v[122:123], v[118:119]\n"
    "v_cvt_pk_bf16_f32 v122, v122, v123\n"
    "v_accvgpr_write_b32 a21, v122\n"
    "v_lshlrev_b32 v120, 16, v22\n"
    "v_and_b32 v121, 0xffff0000, v22\n"
    "v_pk_mul_f32 v[120:121], v[120:121], v[118:119]\n"
    "v_cvt_pk_bf16_f32 v120, v120, v121\n"
    "v_accvgpr_write_b32 a22, v120\n"
    "v_lshlrev_b32 v122, 16, v23\n"
    "v_and_b32 v123, 0xffff0000, v23\n"
    "v_pk_mul_f32 v[122:123], v[122:123], v[118:119]\n"
    "v_cvt_pk_bf16_f32 v122, v122, v123\n"
    "v_accvgpr_write_b32 a23, v122\n"
    "buffer_load_dwordx4 v76, s[24:27], s74 offen offset:1024 nt lds\n"
    "s_waitcnt lgkmcnt(1)\n"
    "v_lshlrev_b32 v120, 16, v24\n"
    "v_and_b32 v121, 0xffff0000, v24\n"
    "v_pk_mul_f32 v[120:121], v[120:121], v[118:119]\n"
    "v_cvt_pk_bf16_f32 v120, v120, v121\n"
    "v_accvgpr_write_b32 a24, v120\n"
    "v_lshlrev_b32 v122, 16, v25\n"
    "v_and_b32 v123, 0xffff0000, v25\n"
    "v_pk_mul_f32 v[122:123], v[122:123], v[118:119]\n"
    "v_cvt_pk_bf16_f32 v122, v122, v123\n"
    "v_accvgpr_write_b32 a25, v122\n"
    "v_lshlrev_b32 v120, 16, v26\n"
    "v_and_b32 v121, 0xffff0000, v26\n"
    "v_pk_mul_f32 v[120:121], v[120:121], v[118:119]\n"
    "v_cvt_pk_bf16_f32 v120, v120, v121\n"
    "v_accvgpr_write_b32 a26, v120\n"
    "v_lshlrev_b32 v122, 16, v27\n"
    "v_and_b32 v123, 0xffff0000, v27\n"
    "v_pk_mul_f32 v[122:123], v[122:123], v[118:119]\n"
    "v_cvt_pk_bf16_f32 v122, v122, v123\n"
    "v_accvgpr_write_b32 a27, v122\n"
    "buffer_load_dwordx4 v77, s[24:27], s74 offen offset:2048 nt lds\n"
    "s_waitcnt lgkmcnt(0)\n"
    "v_lshlrev_b32 v120, 16, v28\n"
    "v_and_b32 v121, 0xffff0000, v28\n"
    "v_pk_mul_f32 v[120:121], v[120:121], v[118:119]\n"
    "v_cvt_pk_bf16_f32 v120, v120, v121\n"
    "v_accvgpr_write_b32 a28, v120\n"
    "v_lshlrev_b32 v122, 16, v29\n"
    "v_and_b32 v123, 0xffff0000, v29\n"
    "v_pk_mul_f32 v[122:123], v[122:123], v[118:119]\n"
    "v_cvt_pk_bf16_f32 v122, v122, v123\n"
    "v_accvgpr_write_b32 a29, v122\n"
    "v_lshlrev_b32 v120, 16, v30\n"
    "v_and_b32 v121, 0xffff0000, v30\n"
    "v_pk_mul_f32 v[120:121], v[120:121], v[118:119]\n"
    "v_cvt_pk_bf16_f32 v120, v120, v121\n"
    "v_accvgpr_write_b32 a30, v120\n"
    "v_lshlrev_b32 v122, 16, v31\n"
    "v_and_b32 v123, 0xffff0000, v31\n"
    "v_pk_mul_f32 v[122:123], v[122:123], v[118:119]\n"
    "v_cvt_pk_bf16_f32 v122, v122, v123\n"
    "v_accvgpr_write_b32 a31, v122\n"
    "buffer_load_dwordx4 v78, s[24:27], s74 offen offset:3072 nt lds\n"
    "s_waitcnt vmcnt(16)\n"
    "ds_read_b128 v[0:3], v114 offset:0\n"
    "ds_read_b128 v[4:7], v115 offset:0\n"
    "ds_read_b128 v[8:11], v114 offset:1024\n"
    "ds_read_b128 v[12:15], v115 offset:1024\n"
    "ds_read_b128 v[16:19], v114 offset:2048\n"
    "ds_read_b128 v[20:23], v115 offset:2048\n"
    "ds_read_b128 v[24:27], v114 offset:3072\n"
    "ds_read_b128 v[28:31], v115 offset:3072\n"
    "s_waitcnt lgkmcnt(7)\n"
    "v_lshlrev_b32 v120, 16, v0\n"
    "v_and_b32 v121, 0xffff0000, v0\n"
    "v_pk_mul_f32 v[120:121], v[120:121], v[118:119]\n"
    "v_cvt_pk_bf16_f32 v120, v120, v121\n"
    "v_accvgpr_write_b32 a32, v120\n"
    "v_lshlrev_b32 v122, 16, v1\n"
    "v_and_b32 v123, 0xffff0000, v1\n"
    "v_pk_mul_f32 v[122:123], v[122:123], v[118:119]\n"
    "v_cvt_pk_bf16_f32 v122, v122, v123\n"
    "v_accvgpr_write_b32 a33, v122\n"
    "v_lshlrev_b32 v120, 16, v2\n"
    "v_and_b32 v121, 0xffff0000, v2\n"
    "v_pk_mul_f32 v[120:121], v[120:121], v[118:119]\n"
    "v_cvt_pk_bf16_f32 v120, v120, v121\n"
    "v_accvgpr_write_b32 a34, v120\n"
    "v_lshlrev_b32 v122, 16, v3\n"
    "v_and_b32 v123, 0xffff0000, v3\n"
    "v_pk_mul_f32 v[122:123], v[122:123], v[118:119]\n"
    "v_cvt_pk_bf16_f32 v122, v122, v123\n"
    "v_accvgpr_write_b32 a35, v122\n"
    "s_add_u32 m0, s46, 16384\n"
    "s_nop 0\n"
    "buffer_load_dwordx4 v79, s[28:31], s73 offen offset:0 nt lds\n"
    "s_waitcnt lgkmcnt(6)\n"
    "v_lshlrev_b32 v120, 16, v4\n"
    "v_and_b32 v121, 0xffff0000, v4\n"
    "v_pk_mul_f32 v[120:121], v[120:121], v[118:119]\n"
    "v_cvt_pk_bf16_f32 v120, v120, v121\n"
    "v_accvgpr_write_b32 a36, v120\n"
    "v_lshlrev_b32 v122, 16, v5\n"
    "v_and_b32 v123, 0xffff0000, v5\n"
    "v_pk_mul_f32 v[122:123], v[122:123], v[118:119]\n"
    "v_cvt_pk_bf16_f32 v122, v122, v123\n"
    "v_accvgpr_write_b32 a37, v122\n"
    "v_lshlrev_b32 v120, 16, v6\n"
    "v_and_b32 v121, 0xffff0000, v6\n"
    "v_pk_mul_f32 v[120:121], v[120:121], v[118:119]\n"
    "v_cvt_pk_bf16_f32 v120, v120, v121\n"
    "v_accvgpr_write_b32 a38, v120\n"
    "v_lshlrev_b32 v122, 16, v7\n"
    "v_and_b32 v123, 0xffff0000, v7\n"
    "v_pk_mul_f32 v[122:123], v[122:123], v[118:119]\n"
    "v_cvt_pk_bf16_f32 v122, v122, v123\n"
    "v_accvgpr_write_b32 a39, v122\n"
    "buffer_load_dwordx4 v80, s[28:31], s73 offen offset:1024 nt lds\n"
    "s_waitcnt lgkmcnt(5)\n"
    "v_lshlrev_b32 v120, 16, v8\n"
    "v_and_b32 v121, 0xffff0000, v8\n"
    "v_pk_mul_f32 v[120:121], v[120:121], v[118:119]\n"
    "v_cvt_pk_bf16_f32 v120, v120, v121\n"
    "v_accvgpr_write_b32 a40, v120\n"
    "v_lshlrev_b32 v122, 16, v9\n"
    "v_and_b32 v123, 0xffff0000, v9\n"
    "v_pk_mul_f32 v[122:123], v[122:123], v[118:119]\n"
    "v_cvt_pk_bf16_f32 v122, v122, v123\n"
    "v_accvgpr_write_b32 a41, v122\n"
    "v_lshlrev_b32 v120, 16, v10\n"
    "v_and_b32 v121, 0xffff0000, v10\n"
    "v_pk_mul_f32 v[120:121], v[120:121], v[118:119]\n"
    "v_cvt_pk_bf16_f32 v120, v120, v121\n"
    "v_accvgpr_write_b32 a42, v120\n"
    "v_lshlrev_b32 v122, 16, v11\n"
    "v_and_b32 v123, 0xffff0000, v11\n"
    "v_pk_mul_f32 v[122:123], v[122:123], v[118:119]\n"
    "v_cvt_pk_bf16_f32 v122, v122, v123\n"
    "v_accvgpr_write_b32 a43, v122\n"
    "buffer_load_dwordx4 v81, s[28:31], s73 offen offset:2048 nt lds\n"
    "s_waitcnt lgkmcnt(4)\n"
    "v_lshlrev_b32 v120, 16, v12\n"
    "v_and_b32 v121, 0xffff0000, v12\n"
    "v_pk_mul_f32 v[120:121], v[120:121], v[118:119]\n"
    "v_cvt_pk_bf16_f32 v120, v120, v121\n"
    "v_accvgpr_write_b32 a44, v120\n"
    "v_lshlrev_b32 v122, 16, v13\n"
    "v_and_b32 v123, 0xffff0000, v13\n"
    "v_pk_mul_f32 v[122:123], v[122:123], v[118:119]\n"
    "v_cvt_pk_bf16_f32 v122, v122, v123\n"
    "v_accvgpr_write_b32 a45, v122\n"
    "v_lshlrev_b32 v120, 16, v14\n"
    "v_and_b32 v121, 0xffff0000, v14\n"
    "v_pk_mul_f32 v[120:121], v[120:121], v[118:119]\n"
    "v_cvt_pk_bf16_f32 v120, v120, v121\n"
    "v_accvgpr_write_b32 a46, v120\n"
    "v_lshlrev_b32 v122, 16, v15\n"
    "v_and_b32 v123, 0xffff0000, v15\n"
    "v_pk_mul_f32 v[122:123], v[122:123], v[118:119]\n"
    "v_cvt_pk_bf16_f32 v122, v122, v123\n"
    "v_accvgpr_write_b32 a47, v122\n"
    "buffer_load_dwordx4 v82, s[28:31], s73 offen offset:3072 nt lds\n"
    "s_waitcnt lgkmcnt(3)\n"
    "v_lshlrev_b32 v120, 16, v16\n"
    "v_and_b32 v121, 0xffff0000, v16\n"
    "v_pk_mul_f32 v[120:121], v[120:121], v[118:119]\n"
    "v_cvt_pk_bf16_f32 v120, v120, v121\n"
    "v_accvgpr_write_b32 a48, v120\n"
    "v_lshlrev_b32 v122, 16, v17\n"
    "v_and_b32 v123, 0xffff0000, v17\n"
    "v_pk_mul_f32 v[122:123], v[122:123], v[118:119]\n"
    "v_cvt_pk_bf16_f32 v122, v122, v123\n"
    "v_accvgpr_write_b32 a49, v122\n"
    "v_lshlrev_b32 v120, 16, v18\n"
    "v_and_b32 v121, 0xffff0000, v18\n"
    "v_pk_mul_f32 v[120:121], v[120:121], v[118:119]\n"
    "v_cvt_pk_bf16_f32 v120, v120, v121\n"
    "v_accvgpr_write_b32 a50, v120\n"
    "v_lshlrev_b32 v122, 16, v19\n"
    "v_and_b32 v123, 0xffff0000, v19\n"
    "v_pk_mul_f32 v[122:123], v[122:123], v[118:119]\n"
    "v_cvt_pk_bf16_f32 v122, v122, v123\n"
    "v_accvgpr_write_b32 a51, v122\n"
    "s_add_u32 m0, s45, 49152\n"
    "s_nop 0\n"
    "buffer_load_dwordx4 v75, s[24:27], s75 offen offset:0 nt lds\n"
    "s_waitcnt lgkmcnt(2)\n"
    "v_lshlrev_b32 v120, 16, v20\n"
    "v_and_b32 v121, 0xffff0000, v20\n"
    "v_pk_mul_f32 v[120:121], v[120:121], v[118:119]\n"
    "v_cvt_pk_bf16_f32 v120, v120, v121\n"
    "v_accvgpr_write_b32 a52, v120\n"
    "v_lshlrev_b32 v122, 16, v21\n"
    "v_and_b32 v123, 0xffff0000, v21\n"
    "v_pk_mul_f32 v[122:123], v[122:123], v[118:119]\n"
    "v_cvt_pk_bf16_f32 v122, v122, v123\n"
    "v_accvgpr_write_b32 a53, v122\n"
    "v_lshlrev_b32 v120, 16, v22\n"
    "v_and_b32 v121, 0xffff0000, v22\n"
    "v_pk_mul_f32 v[120:121], v[120:121], v[118:119]\n"
    "v_cvt_pk_bf16_f32 v120, v120, v121\n"
    "v_accvgpr_write_b32 a54, v120\n"
    "v_lshlrev_b32 v122, 16, v23\n"
    "v_and_b32 v123, 0xffff0000, v23\n"
    "v_pk_mul_f32 v[122:123], v[122:123], v[118:119]\n"
    "v_cvt_pk_bf16_f32 v122, v122, v123\n"
    "v_accvgpr_write_b32 a55, v122\n"
    "buffer_load_dwordx4 v76, s[24:27], s75 offen offset:1024 nt lds\n"
    "s_waitcnt lgkmcnt(1)\n"
    "v_lshlrev_b32 v120, 16, v24\n"
    "v_and_b32 v121, 0xffff0000, v24\n"
    "v_pk_mul_f32 v[120:121], v[120:121], v[118:119]\n"
    "v_cvt_pk_bf16_f32 v120, v120, v121\n"
    "v_accvgpr_write_b32 a56, v120\n"
    "v_lshlrev_b32 v122, 16, v25\n"
    "v_and_b32 v123, 0xffff0000, v25\n"
    "v_pk_mul_f32 v[122:123], v[122:123], v[118:119]\n"
    "v_cvt_pk_bf16_f32 v122, v122, v123\n"
    "v_accvgpr_write_b32 a57, v122\n"
    "v_lshlrev_b32 v120, 16, v26\n"
    "v_and_b32 v121, 0xffff0000, v26\n"
    "v_pk_mul_f32 v[120:121], v[120:121], v[118:119]\n"
    "v_cvt_pk_bf16_f32 v120, v120, v121\n"
    "v_accvgpr_write_b32 a58, v120\n"
    "v_lshlrev_b32 v122, 16, v27\n"
    "v_and_b32 v123, 0xffff0000, v27\n"
    "v_pk_mul_f32 v[122:123], v[122:123], v[118:119]\n"
    "v_cvt_pk_bf16_f32 v122, v122, v123\n"
    "v_accvgpr_write_b32 a59, v122\n"
    "buffer_load_dwordx4 v77, s[24:27], s75 offen offset:2048 nt lds\n"
    "s_waitcnt lgkmcnt(0)\n"
    "buffer_load_dwordx4 v78, s[24:27], s75 offen offset:3072 nt lds\n"
    "s_add_u32 m0, s46, 32768\n"
    "s_nop 0\n"
    "buffer_load_dwordx4 v79, s[28:31], s74 offen offset:0 nt lds\n"
    "buffer_load_dwordx4 v80, s[28:31], s74 offen offset:1024 nt lds\n"
    "buffer_load_dwordx4 v81, s[28:31], s74 offen offset:2048 nt lds\n"
    "buffer_load_dwordx4 v82, s[28:31], s74 offen offset:3072 nt lds\n"
    "v_lshlrev_b32 v120, 16, v28\n"
    "v_and_b32 v121, 0xffff0000, v28\n"
    "v_pk_mul_f32 v[120:121], v[120:121], v[118:119]\n"
    "v_cvt_pk_bf16_f32 v120, v120, v121\n"
    "v_accvgpr_write_b32 a60, v120\n"
    "v_lshlrev_b32 v122, 16, v29\n"
    "v_and_b32 v123, 0xffff0000, v29\n"
    "v_pk_mul_f32 v[122:123], v[122:123], v[118:119]\n"
    "v_cvt_pk_bf16_f32 v122, v122, v123\n"
    "v_accvgpr_write_b32 a61, v122\n"
    "v_lshlrev_b32 v120, 16, v30\n"
    "v_and_b32 v121, 0xffff0000, v30\n"
    "v_pk_mul_f32 v[120:121], v[120:121], v[118:119]\n"
    "v_cvt_pk_bf16_f32 v120, v120, v121\n"
    "v_accvgpr_write_b32 a62, v120\n"
    "v_lshlrev_b32 v122, 16, v31\n"
    "v_and_b32 v123, 0xffff0000, v31\n"
    "v_pk_mul_f32 v[122:123], v[122:123], v[118:119]\n"
    "v_cvt_pk_bf16_f32 v122, v122, v123\n"
    "v_accvgpr_write_b32 a63, v122\n"
    "s_waitcnt vmcnt(20)\n"
    "s_barrier\n"
    "ds_read_b128 a[64:67], v69 offset:0\n"
    "ds_read_b128 a[68:71], v70 offset:0\n"
    "ds_read_b128 a[72:75], v69 offset:1024\n"
    "ds_read_b128 a[76:79], v70 offset:1024\n"
    "ds_read_b128 a[80:83], v69 offset:2048\n"
    "ds_read_b128 a[84:87], v70 offset:2048\n"
    "ds_read_b128 a[88:91], v69 offset:3072\n"
    "ds_read_b128 a[92:95], v70 offset:3072\n"
    "ds_read_b128 a[96:99], v69 offset:8192\n"
    "ds_read_b128 a[100:103], v70 offset:8192\n"
    "ds_read_b128 a[104:107], v69 offset:9216\n"
    "ds_read_b128 a[108:111], v70 offset:9216\n"
    "ds_read_b128 a[112:115], v69 offset:10240\n"
    "ds_read_b128 a[116:119], v70 offset:10240\n"
    "ds_read_b128 a[120:123], v69 offset:11264\n"
    "ds_read_b128 a[124:127], v70 offset:11264\n"
    "s_waitcnt lgkmcnt(15)\n"
    "v_mfma_f32_16x16x32_bf16 v[0:3], a[64:67], a[0:3], v[32:35]\n"
    "s_waitcnt lgkmcnt(14)\n"
    "v_mfma_f32_16x16x32_bf16 v[0:3], a[68:71], a[4:7], v[0:3]\n"
    "s_waitcnt lgkmcnt(13)\n"
    "v_mfma_f32_16x16x32_bf16 v[0:3], a[72:75], a[8:11], v[0:3]\n"
    "s_waitcnt lgkmcnt(12)\n"
    "v_mfma_f32_16x16x32_bf16 v[0:3], a[76:79], a[12:15], v[0:3]\n"
    "s_waitcnt lgkmcnt(11)\n"
    "v_mfma_f32_16x16x32_bf16 v[0:3], a[80:83], a[16:19], v[0:3]\n"
    "s_waitcnt lgkmcnt(10)\n"
    "v_mfma_f32_16x16x32_bf16 v[0:3], a[84:87], a[20:23], v[0:3]\n"
    "s_waitcnt lgkmcnt(9)\n"
    "v_mfma_f32_16x16x32_bf16 v[0:3], a[88:91], a[24:27], v[0:3]\n"
    "s_waitcnt lgkmcnt(8)\n"
    "v_mfma_f32_16x16x32_bf16 v[0:3], a[92:95], a[28:31], v[0:3]\n"
    "s_waitcnt lgkmcnt(7)\n"
    "v_mfma_f32_16x16x32_bf16 v[8:11], a[96:99], a[0:3], v[40:43]\n"
    "s_waitcnt lgkmcnt(6)\n"
    "v_mfma_f32_16x16x32_bf16 v[8:11], a[100:103], a[4:7], v[8:11]\n"
    "s_waitcnt lgkmcnt(5)\n"
    "v_mfma_f32_16x16x32_bf16 v[8:11], a[104:107], a[8:11], v[8:11]\n"
    "s_waitcnt lgkmcnt(4)\n"
    "v_mfma_f32_16x16x32_bf16 v[8:11], a[108:111], a[12:15], v[8:11]\n"
    "s_waitcnt lgkmcnt(3)\n"
    "v_mfma_f32_16x16x32_bf16 v[8:11], a[112:115], a[16:19], v[8:11]\n"
    "s_waitcnt lgkmcnt(2)\n"
    "v_mfma_f32_16x16x32_bf16 v[8:11], a[116:119], a[20:23], v[8:11]\n"
    "s_waitcnt lgkmcnt(1)\n"
    "v_mfma_f32_16x16x32_bf16 v[8:11], a[120:123], a[24:27], v[8:11]\n"
    "s_waitcnt lgkmcnt(0)\n"
    "v_mfma_f32_16x16x32_bf16 v[8:11], a[124:127], a[28:31], v[8:11]\n"
    "v_mfma_f32_16x16x32_bf16 v[4:7], a[64:67], a[32:35], v[36:39]\n"
    "v_mfma_f32_16x16x32_bf16 v[4:7], a[68:71], a[36:39], v[4:7]\n"
    "v_mfma_f32_16x16x32_bf16 v[4:7], a[72:75], a[40:43], v[4:7]\n"
    "v_mfma_f32_16x16x32_bf16 v[4:7], a[76:79], a[44:47], v[4:7]\n"
    "v_mfma_f32_16x16x32_bf16 v[4:7], a[80:83], a[48:51], v[4:7]\n"
    "v_mfma_f32_16x16x32_bf16 v[4:7], a[84:87], a[52:55], v[4:7]\n"
    "v_mfma_f32_16x16x32_bf16 v[4:7], a[88:91], a[56:59], v[4:7]\n"
    "v_mfma_f32_16x16x32_bf16 v[4:7], a[92:95], a[60:63], v[4:7]\n"
    "v_mfma_f32_16x16x32_bf16 v[12:15], a[96:99], a[32:35], v[44:47]\n"
    "ds_read_b128 a[64:67], v69 offset:16384\n"
    "v_mfma_f32_16x16x32_bf16 v[12:15], a[100:103], a[36:39], v[12:15]\n"
    "ds_read_b128 a[68:71], v70 offset:16384\n"
    "v_mfma_f32_16x16x32_bf16 v[12:15], a[104:107], a[40:43], v[12:15]\n"
    "ds_read_b128 a[72:75], v69 offset:17408\n"
    "v_mfma_f32_16x16x32_bf16 v[12:15], a[108:111], a[44:47], v[12:15]\n"
    "ds_read_b128 a[76:79], v70 offset:17408\n"
    "v_mfma_f32_16x16x32_bf16 v[12:15], a[112:115], a[48:51], v[12:15]\n"
    "ds_read_b128 a[80:83], v69 offset:18432\n"
    "v_mfma_f32_16x16x32_bf16 v[12:15], a[116:119], a[52:55], v[12:15]\n"
    "ds_read_b128 a[84:87], v70 offset:18432\n"
    "v_mfma_f32_16x16x32_bf16 v[12:15], a[120:123], a[56:59], v[12:15]\n"
    "ds_read_b128 a[88:91], v69 offset:19456\n"
    "v_mfma_f32_16x16x32_bf16 v[12:15], a[124:127], a[60:63], v[12:15]\n"
    "ds_read_b128 a[92:95], v70 offset:19456\n"
    "ds_read_b128 a[96:99], v69 offset:24576\n"
    "ds_read_b128 a[100:103], v70 offset:24576\n"
    "ds_read_b128 a[104:107], v69 offset:25600\n"
    "ds_read_b128 a[108:111], v70 offset:25600\n"
    "ds_read_b128 a[112:115], v69 offset:26624\n"
    "ds_read_b128 a[116:119], v70 offset:26624\n"
    "ds_read_b128 a[120:123], v69 offset:27648\n"
    "ds_read_b128 a[124:127], v70 offset:27648\n"
    "v_max3_f32 v120, v0, v1, v2\n"
    "v_max3_f32 v121, v4, v5, v6\n"
    "v_max3_f32 v120, v120, v3, v8\n"
    "v_max3_f32 v121, v121, v7, v12\n"
    "v_max3_f32 v120, v120, v9, v10\n"
    "v_max3_f32 v121, v121, v13, v14\n"
    "v_max_f32 v120, v120, v11\n"
    "v_max_f32 v121, v121, v15\n"
    "v_mov_b32 v122, v120\n"
    "v_mov_b32 v123, v121\n"
    "s_nop 0\n"
    "v_permlane16_swap_b32 v120, v122\n"
    "v_permlane16_swap_b32 v121, v123\n"
    "s_nop 0\n"
    "v_max_f32 v120, v120, v122\n"
    "v_max_f32 v121, v121, v123\n"
    "v_mov_b32 v122, v120\n"
    "v_mov_b32 v123, v121\n"
    "s_nop 0\n"
    "v_permlane32_swap_b32 v120, v122\n"
    "v_permlane32_swap_b32 v121, v123\n"
    "s_nop 0\n"
    "v_max_f32 v120, v120, v122\n"
    "v_max_f32 v121, v121, v123\n"
    "v_sub_f32 v16, v0, v120\n"
    "v_sub_f32 v17, v1, v120\n"
    "v_sub_f32 v18, v2, v120\n"
    "v_sub_f32 v19, v3, v120\n"
    "v_sub_f32 v20, v4, v121\n"
    "v_sub_f32 v21, v5, v121\n"
    "v_sub_f32 v22, v6, v121\n"
    "v_sub_f32 v23, v7, v121\n"
    "v_sub_f32 v24, v8, v120\n"
    "v_sub_f32 v25, v9, v120\n"
    "v_sub_f32 v26, v10, v120\n"
    "v_sub_f32 v27, v11, v120\n"
    "v_sub_f32 v28, v12, v121\n"
    "v_sub_f32 v29, v13, v121\n"
    "v_sub_f32 v30, v14, v121\n"
    "v_sub_f32 v31, v15, v121\n"
    "v_exp_f32 v16, v16\n"
    "v_exp_f32 v17, v17\n"
    "v_exp_f32 v18, v18\n"
    "v_exp_f32 v19, v19\n"
    "v_exp_f32 v20, v20\n"
    "v_exp_f32 v21, v21\n"
    "v_exp_f32 v22, v22\n"
    "v_exp_f32 v23, v23\n"
    "v_exp_f32 v24, v24\n"
    "v_exp_f32 v25, v25\n"
    "v_exp_f32 v26, v26\n"
    "v_exp_f32 v27, v27\n"
    "v_exp_f32 v28, v28\n"
    "v_exp_f32 v29, v29\n"
    "v_exp_f32 v30, v30\n"
    "v_exp_f32 v31, v31\n"
    "v_sub_f32 v32, v32, v120\n"
    "v_sub_f32 v33, v33, v120\n"
    "v_sub_f32 v34, v34, v120\n"
    "v_sub_f32 v35, v35, v120\n"
    "v_sub_f32 v36, v36, v121\n"
    "v_sub_f32 v37, v37, v121\n"
    "v_sub_f32 v38, v38, v121\n"
    "v_sub_f32 v39, v39, v121\n"
    "v_sub_f32 v40, v40, v120\n"
    "v_sub_f32 v41, v41, v120\n"
    "v_sub_f32 v42, v42, v120\n"
    "v_sub_f32 v43, v43, v120\n"
    "v_sub_f32 v44, v44, v121\n"
    "v_sub_f32 v45, v45, v121\n"
    "v_sub_f32 v46, v46, v121\n"
    "v_sub_f32 v47, v47, v121\n"
    "v_add_f32 v64, v16, v17\n"
    "v_add_f32 v65, v20, v21\n"
    "v_add_f32 v64, v64, v18\n"
    "v_add_f32 v65, v65, v22\n"
    "v_add_f32 v64, v64, v19\n"
    "v_add_f32 v65, v65, v23\n"
    "v_add_f32 v64, v64, v24\n"
    "v_add_f32 v65, v65, v28\n"
    "v_add_f32 v64, v64, v25\n"
    "v_add_f32 v65, v65, v29\n"
    "v_add_f32 v64, v64, v26\n"
    "v_add_f32 v65, v65, v30\n"
    "v_add_f32 v64, v64, v27\n"
    "v_add_f32 v65, v65, v31\n"
    "v_cvt_pk_bf16_f32 v48, v16, v17\n"
    "v_cvt_pk_bf16_f32 v49, v18, v19\n"
    "v_cvt_pk_bf16_f32 v52, v20, v21\n"
    "v_cvt_pk_bf16_f32 v53, v22, v23\n"
    "v_cvt_pk_bf16_f32 v50, v24, v25\n"
    "v_cvt_pk_bf16_f32 v51, v26, v27\n"
    "v_cvt_pk_bf16_f32 v54, v28, v29\n"
    "v_cvt_pk_bf16_f32 v55, v30, v31\n"
    "s_waitcnt vmcnt(12)\n"
    "s_barrier\n"
    "s_cmp_le_u32 s35, 3\n"
    "s_cbranch_scc1 .Lstub3_noslow\n"
    ".p2align 6\n"
    ".Ltop0:\n"
    "v_mfma_f32_16x16x32_bf16 v[0:3], a[64:67], a[0:3], v[32:35]\n"
    "s_add_u32 m0, s45, 0\n"
    "ds_read_b64_tr_b16 a[128:129], v73 offset:0\n"
    "ds_read_b64_tr_b16 a[130:131], v73 offset:8192\n"
    "v_mfma_f32_16x16x32_bf16 v[0:3], a[68:71], a[4:7], v[0:3]\n"
    "buffer_load_dwordx4 v75, s[24:27], s43 offen offset:0 nt lds\n"
    "v_mfma_f32_16x16x32_bf16 v[0:3], a[72:75], a[8:11], v[0:3]\n"
    "ds_read_b64_tr_b16 a[132:133], v73 offset:256\n"
    "ds_read_b64_tr_b16 a[134:135], v73 offset:8448\n"
    "v_mfma_f32_16x16x32_bf16 v[0:3], a[76:79], a[12:15], v[0:3]\n"
    "buffer_load_dwordx4 v76, s[24:27], s43 offen offset:1024 nt lds\n"
    "v_mfma_f32_16x16x32_bf16 v[0:3], a[80:83], a[16:19], v[0:3]\n"
    "ds_read_b64_tr_b16 a[136:137], v73 offset:512\n"
    "ds_read_b64_tr_b16 a[138:139], v73 offset:8704\n"
    "v_mfma_f32_16x16x32_bf16 v[0:3], a[84:87], a[20:23], v[0:3]\n"
    "buffer_load_dwordx4 v77, s[24:27], s43 offen offset:2048 nt lds\n"
    "v_mfma_f32_16x16x32_bf16 v[0:3], a[88:91], a[24:27], v[0:3]\n"
    "ds_read_b64_tr_b16 a[140:141], v73 offset:768\n"
    "ds_read_b64_tr_b16 a[142:143], v73 offset:8960\n"
    "v_mfma_f32_16x16x32_bf16 v[0:3], a[92:95], a[28:31], v[0:3]\n"
    "buffer_load_dwordx4 v78, s[24:27], s43 offen offset:3072 nt lds\n"
    "v_mfma_f32_16x16x32_bf16 v[4:7], a[64:67], a[32:35], v[36:39]\n"
    "s_add_u32 s42, s42, 1\n"
    "ds_read_b64_tr_b16 a[144:145], v73 offset:1024\n"
    "ds_read_b64_tr_b16 a[146:147], v73 offset:9216\n"
    "v_mfma_f32_16x16x32_bf16 v[4:7], a[68:71], a[36:39], v[4:7]\n"
    "v_cvt_pk_bf16_f32 v54, v28, v29\n"
    "v_cvt_pk_bf16_f32 v55, v30, v31\n"
    "v_mfma_f32_16x16x32_bf16 v[4:7], a[72:75], a[40:43], v[4:7]\n"
    "ds_read_b64_tr_b16 a[148:149], v73 offset:1280\n"
    "ds_read_b64_tr_b16 a[150:151], v73 offset:9472\n"
    "v_mfma_f32_16x16x32_bf16 v[4:7], a[76:79], a[44:47], v[4:7]\n"
    "v_exp_f32 v16, v0\n"
    "v_mfma_f32_16x16x32_bf16 v[4:7], a[80:83], a[48:51], v[4:7]\n"
    "ds_read_b64_tr_b16 a[152:153], v73 offset:1536\n"
    "ds_read_b64_tr_b16 a[154:155], v73 offset:9728\n"
    "v_mfma_f32_16x16x32_bf16 v[4:7], a[84:87], a[52:55], v[4:7]\n"
    "v_exp_f32 v17, v1\n"
    "v_mfma_f32_16x16x32_bf16 v[4:7], a[88:91], a[56:59], v[4:7]\n"
    "ds_read_b64_tr_b16 a[156:157], v73 offset:1792\n"
    "ds_read_b64_tr_b16 a[158:159], v73 offset:9984\n"
    "v_mfma_f32_16x16x32_bf16 v[4:7], a[92:95], a[60:63], v[4:7]\n"
    "s_waitcnt lgkmcnt(12)\n"
    "v_exp_f32 v18, v2\n"
    "v_mfma_f32_16x16x32_bf16 v[8:11], a[96:99], a[0:3], v[40:43]\n"
    "ds_read_b64_tr_b16 a[160:161], v73 offset:2048\n"
    "ds_read_b64_tr_b16 a[162:163], v73 offset:10240\n"
    "v_mfma_f32_16x16x32_bf16 v[8:11], a[100:103], a[4:7], v[8:11]\n"
    "v_exp_f32 v19, v3\n"
    "v_mfma_f32_16x16x32_bf16 v[8:11], a[104:107], a[8:11], v[8:11]\n"
    "ds_read_b64_tr_b16 a[164:165], v73 offset:2304\n"
    "ds_read_b64_tr_b16 a[166:167], v73 offset:10496\n"
    "v_mfma_f32_16x16x32_bf16 v[8:11], a[108:111], a[12:15], v[8:11]\n"
    "v_exp_f32 v20, v4\n"
    "v_mfma_f32_16x16x32_bf16 v[8:11], a[112:115], a[16:19], v[8:11]\n"
    "ds_read_b64_tr_b16 a[168:169], v73 offset:2560\n"
    "ds_read_b64_tr_b16 a[170:171], v73 offset:10752\n"
    "v_mfma_f32_16x16x32_bf16 v[8:11], a[116:119], a[20:23], v[8:11]\n"
    "v_exp_f32 v21, v5\n"
    "v_mfma_f32_16x16x32_bf16 v[8:11], a[120:123], a[24:27], v[8:11]\n"
    "ds_read_b64_tr_b16 a[172:173], v73 offset:2816\n"
    "ds_read_b64_tr_b16 a[174:175], v73 offset:11008\n"
    "v_mfma_f32_16x16x32_bf16 v[8:11], a[124:127], a[28:31], v[8:11]\n"
    "v_exp_f32 v22, v6\n"
    "v_mfma_f32_16x16x32_bf16 v[12:15], a[96:99], a[32:35], v[44:47]\n"
    "ds_read_b64_tr_b16 a[176:177], v73 offset:3072\n"
    "ds_read_b64_tr_b16 a[178:179], v73 offset:11264\n"
    "v_mfma_f32_16x16x32_bf16 v[12:15], a[100:103], a[36:39], v[12:15]\n"
    "v_exp_f32 v23, v7\n"
    "v_mfma_f32_16x16x32_bf16 v[12:15], a[104:107], a[40:43], v[12:15]\n"
    "ds_read_b64_tr_b16 a[180:181], v73 offset:3328\n"
    "ds_read_b64_tr_b16 a[182:183], v73 offset:11520\n"
    "v_mfma_f32_16x16x32_bf16 v[12:15], a[108:111], a[44:47], v[12:15]\n"
    "v_exp_f32 v24, v8\n"
    "v_mfma_f32_16x16x32_bf16 v[12:15], a[112:115], a[48:51], v[12:15]\n"
    "ds_read_b64_tr_b16 a[184:185], v73 offset:3584\n"
    "ds_read_b64_tr_b16 a[186:187], v73 offset:11776\n"
    "v_mfma_f32_16x16x32_bf16 v[12:15], a[116:119], a[52:55], v[12:15]\n"
    "v_exp_f32 v25, v9\n"
    "v_mfma_f32_16x16x32_bf16 v[12:15], a[120:123], a[56:59], v[12:15]\n"
    "ds_read_b64_tr_b16 a[188:189], v73 offset:3840\n"
    "ds_read_b64_tr_b16 a[190:191], v73 offset:12032\n"
    "v_mfma_f32_16x16x32_bf16 v[12:15], a[124:127], a[60:63], v[12:15]\n"
    "s_waitcnt lgkmcnt(8)\n"
    "v_exp_f32 v26, v10\n"
    "v_mfma_f32_16x16x32_bf16 v[128:131], a[128:131], v[48:51], v[128:131]\n"
    "ds_read_b128 a[64:67], v69 offset:32768\n"
    "v_add_f32 v66, v64, v16\n"
    "v_mfma_f32_16x16x32_bf16 v[132:135], a[128:131], v[52:55], v[132:135]\n"
    "v_exp_f32 v27, v11\n"
    "v_mfma_f32_16x16x32_bf16 v[136:139], a[132:135], v[48:51], v[136:139]\n"
    "ds_read_b128 a[68:71], v70 offset:32768\n"
    "v_add_f32 v66, v66, v17\n"
    "v_mfma_f32_16x16x32_bf16 v[140:143], a[132:135], v[52:55], v[140:143]\n"
    "v_exp_f32 v28, v12\n"
    "v_mfma_f32_16x16x32_bf16 v[144:147], a[136:139], v[48:51], v[144:147]\n"
    "ds_read_b128 a[72:75], v69 offset:33792\n"
    "v_add_f32 v66, v66, v18\n"
    "v_mfma_f32_16x16x32_bf16 v[148:151], a[136:139], v[52:55], v[148:151]\n"
    "v_exp_f32 v29, v13\n"
    "v_mfma_f32_16x16x32_bf16 v[152:155], a[140:143], v[48:51], v[152:155]\n"
    "ds_read_b128 a[76:79], v70 offset:33792\n"
    "v_add_f32 v66, v66, v19\n"
    "v_mfma_f32_16x16x32_bf16 v[156:159], a[140:143], v[52:55], v[156:159]\n"
    "v_exp_f32 v30, v14\n"
    "v_mfma_f32_16x16x32_bf16 v[160:163], a[144:147], v[48:51], v[160:163]\n"
    "ds_read_b128 a[80:83], v69 offset:34816\n"
    "v_add_f32 v67, v65, v20\n"
    "v_mfma_f32_16x16x32_bf16 v[164:167], a[144:147], v[52:55], v[164:167]\n"
    "v_exp_f32 v31, v15\n"
    "v_mfma_f32_16x16x32_bf16 v[168:171], a[148:151], v[48:51], v[168:171]\n"
    "s_add_u32 m0, s46, 49152\n"
    "ds_read_b128 a[84:87], v70 offset:34816\n"
    "v_add_f32 v67, v67, v21\n"
    "v_mfma_f32_16x16x32_bf16 v[172:175], a[148:151], v[52:55], v[172:175]\n"
    "buffer_load_dwordx4 v79, s[28:31], s44 offen offset:0 nt lds\n"
    "v_mfma_f32_16x16x32_bf16 v[176:179], a[152:155], v[48:51], v[176:179]\n"
    "ds_read_b128 a[88:91], v69 offset:35840\n"
    "v_add_f32 v67, v67, v22\n"
    "v_mfma_f32_16x16x32_bf16 v[180:183], a[152:155], v[52:55], v[180:183]\n"
    "buffer_load_dwordx4 v80, s[28:31], s44 offen offset:1024 nt lds\n"
    "v_mfma_f32_16x16x32_bf16 v[184:187], a[156:159], v[48:51], v[184:187]\n"
    "ds_read_b128 a[92:95], v70 offset:35840\n"
    "v_add_f32 v67, v67, v23\n"
    "v_mfma_f32_16x16x32_bf16 v[188:191], a[156:159], v[52:55], v[188:191]\n"
    "buffer_load_dwordx4 v81, s[28:31], s44 offen offset:2048 nt lds\n"
    "v_mfma_f32_16x16x32_bf16 v[192:195], a[160:163], v[48:51], v[192:195]\n"
    "ds_read_b128 a[96:99], v69 offset:40960\n"
    "v_cvt_pk_bf16_f32 v56, v16, v17\n"
    "v_mfma_f32_16x16x32_bf16 v[196:199], a[160:163], v[52:55], v[196:199]\n"
    "buffer_load_dwordx4 v82, s[28:31], s44 offen offset:3072 nt lds\n"
    "v_mfma_f32_16x16x32_bf16 v[200:203], a[164:167], v[48:51], v[200:203]\n"
    "s_mov_b32 s44, s43\n"
    "ds_read_b128 a[100:103], v70 offset:40960\n"
    "v_cvt_pk_bf16_f32 v57, v18, v19\n"
    "v_mfma_f32_16x16x32_bf16 v[204:207], a[164:167], v[52:55], v[204:207]\n"
    "v_readlane_b32 s43, v83, s42\n"
    "v_mfma_f32_16x16x32_bf16 v[208:211], a[168:171], v[48:51], v[208:211]\n"
    "s_sub_u32 s35, s35, 1\n"
    "ds_read_b128 a[104:107], v69 offset:41984\n"
    "v_add_f32 v66, v66, v24\n"
    "v_mfma_f32_16x16x32_bf16 v[212:215], a[168:171], v[52:55], v[212:215]\n"
    "v_add_f32 v66, v66, v25\n"
    "v_add_f32 v66, v66, v26\n"
    "v_mfma_f32_16x16x32_bf16 v[216:219], a[172:175], v[48:51], v[216:219]\n"
    "s_cmp_le_u32 s35, 3\n"
    "ds_read_b128 a[108:111], v70 offset:41984\n"
    "v_add_f32 v66, v66, v27\n"
    "v_mfma_f32_16x16x32_bf16 v[220:223], a[172:175], v[52:55], v[220:223]\n"
    "s_waitcnt lgkmcnt(12)\n"
    "v_add_f32 v67, v67, v28\n"
    "v_add_f32 v67, v67, v29\n"
    "v_mfma_f32_16x16x32_bf16 v[224:227], a[176:179], v[48:51], v[224:227]\n"
    "s_cselect_b32 s99, s33, s47\n"
    "ds_read_b128 a[112:115], v69 offset:43008\n"
    "v_add_f32 v67, v67, v30\n"
    "v_mfma_f32_16x16x32_bf16 v[228:231], a[176:179], v[52:55], v[228:231]\n"
    "v_add_f32 v67, v67, v31\n"
    "v_cvt_pk_bf16_f32 v60, v20, v21\n"
    "v_mfma_f32_16x16x32_bf16 v[232:235], a[180:183], v[48:51], v[232:235]\n"
    "ds_read_b128 a[116:119], v70 offset:43008\n"
    "v_cvt_pk_bf16_f32 v61, v22, v23\n"
    "v_mfma_f32_16x16x32_bf16 v[236:239], a[180:183], v[52:55], v[236:239]\n"
    "v_cvt_pk_bf16_f32 v58, v24, v25\n"
    "v_cvt_pk_bf16_f32 v59, v26, v27\n"
    "v_mfma_f32_16x16x32_bf16 v[240:243], a[184:187], v[48:51], v[240:243]\n"
    "ds_read_b128 a[120:123], v69 offset:44032\n"
    "v_max_f32 v68, v66, v67\n"
    "v_mfma_f32_16x16x32_bf16 v[244:247], a[184:187], v[52:55], v[244:247]\n"
    "s_waitcnt vmcnt(12)\n"
    "s_barrier\n"
    "v_mfma_f32_16x16x32_bf16 v[248:251], a[188:191], v[48:51], v[248:251]\n"
    "ds_read_b128 a[124:127], v70 offset:44032\n"
    "v_cmp_nge_f32 vcc, s99, v68\n"
    "v_mfma_f32_16x16x32_bf16 v[252:255], a[188:191], v[52:55], v[252:255]\n"
    "s_waitcnt lgkmcnt(8)\n"
    "s_cbranch_vccnz .Lstub0\n"
    ".Ltop1:\n"
    "v_mfma_f32_16x16x32_bf16 v[0:3], a[64:67], a[0:3], v[32:35]\n"
    "s_add_u32 m0, s45, 16384\n"
    "ds_read_b64_tr_b16 a[128:129], v73 offset:16384\n"
    "ds_read_b64_tr_b16 a[130:131], v73 offset:24576\n"
    "v_mfma_f32_16x16x32_bf16 v[0:3], a[68:71], a[4:7], v[0:3]\n"
    "buffer_load_dwordx4 v75, s[24:27], s43 offen offset:0 nt lds\n"
    "v_mfma_f32_16x16x32_bf16 v[0:3], a[72:75], a[8:11], v[0:3]\n"
    "ds_read_b64_tr_b16 a[132:133], v73 offset:16640\n"
    "ds_read_b64_tr_b16 a[134:135], v73 offset:24832\n"
    "v_mfma_f32_16x16x32_bf16 v[0:3], a[76:79], a[12:15], v[0:3]\n"
    "buffer_load_dwordx4 v76, s[24:27], s43 offen offset:1024 nt lds\n"
    "v_mfma_f32_16x16x32_bf16 v[0:3], a[80:83], a[16:19], v[0:3]\n"
    "ds_read_b64_tr_b16 a[136:137], v73 offset:16896\n"
    "ds_read_b64_tr_b16 a[138:139], v73 offset:25088\n"
    "v_mfma_f32_16x16x32_bf16 v[0:3], a[84:87], a[20:23], v[0:3]\n"
    "buffer_load_dwordx4 v77, s[24:27], s43 offen offset:2048 nt lds\n"
    "v_mfma_f32_16x16x32_bf16 v[0:3], a[88:91], a[24:27], v[0:3]\n"
    "ds_read_b64_tr_b16 a[140:141], v73 offset:17152\n"
    "ds_read_b64_tr_b16 a[142:143], v73 offset:25344\n"
    "v_mfma_f32_16x16x32_bf16 v[0:3], a[92:95], a[28:31], v[0:3]\n"
    "buffer_load_dwordx4 v78, s[24:27], s43 offen offset:3072 nt lds\n"
    "v_mfma_f32_16x16x32_bf16 v[4:7], a[64:67], a[32:35], v[36:39]\n"
    "s_add_u32 s42, s42, 1\n"
    "ds_read_b64_tr_b16 a[144:145], v73 offset:17408\n"
    "ds_read_b64_tr_b16 a[146:147], v73 offset:25600\n"
    "v_mfma_f32_16x16x32_bf16 v[4:7], a[68:71], a[36:39], v[4:7]\n"
    "v_cvt_pk_bf16_f32 v62, v28, v29\n"
    "v_cvt_pk_bf16_f32 v63, v30, v31\n"
    "v_mfma_f32_16x16x32_bf16 v[4:7], a[72:75], a[40:43], v[4:7]\n"
    "ds_read_b64_tr_b16 a[148:149], v73 offset:17664\n"
    "ds_read_b64_tr_b16 a[150:151], v73 offset:25856\n"
    "v_mfma_f32_16x16x32_bf16 v[4:7], a[76:79], a[44:47], v[4:7]\n"
    "v_exp_f32 v16, v0\n"
    "v_mfma_f32_16x16x32_bf16 v[4:7], a[80:83], a[48:51], v[4:7]\n"
    "ds_read_b64_tr_b16 a[152:153], v73 offset:17920\n"
    "ds_read_b64_tr_b16 a[154:155], v73 offset:26112\n"
    "v_mfma_f32_16x16x32_bf16 v[4:7], a[84:87], a[52:55], v[4:7]\n"
    "v_exp_f32 v17, v1\n"
    "v_mfma_f32_16x16x32_bf16 v[4:7], a[88:91], a[56:59], v[4:7]\n"
    "ds_read_b64_tr_b16 a[156:157], v73 offset:18176\n"
    "ds_read_b64_tr_b16 a[158:159], v73 offset:26368\n"
    "v_mfma_f32_16x16x32_bf16 v[4:7], a[92:95], a[60:63], v[4:7]\n"
    "s_waitcnt lgkmcnt(12)\n"
    "v_exp_f32 v18, v2\n"
    "v_mfma_f32_16x16x32_bf16 v[8:11], a[96:99], a[0:3], v[40:43]\n"
    "ds_read_b64_tr_b16 a[160:161], v73 offset:18432\n"
    "ds_read_b64_tr_b16 a[162:163], v73 offset:26624\n"
    "v_mfma_f32_16x16x32_bf16 v[8:11], a[100:103], a[4:7], v[8:11]\n"
    "v_exp_f32 v19, v3\n"
    "v_mfma_f32_16x16x32_bf16 v[8:11], a[104:107], a[8:11], v[8:11]\n"
    "ds_read_b64_tr_b16 a[164:165], v73 offset:18688\n"
    "ds_read_b64_tr_b16 a[166:167], v73 offset:26880\n"
    "v_mfma_f32_16x16x32_bf16 v[8:11], a[108:111], a[12:15], v[8:11]\n"
    "v_exp_f32 v20, v4\n"
    "v_mfma_f32_16x16x32_bf16 v[8:11], a[112:115], a[16:19], v[8:11]\n"
    "ds_read_b64_tr_b16 a[168:169], v73 offset:18944\n"
    "ds_read_b64_tr_b16 a[170:171], v73 offset:27136\n"
    "v_mfma_f32_16x16x32_bf16 v[8:11], a[116:119], a[20:23], v[8:11]\n"
    "v_exp_f32 v21, v5\n"
    "v_mfma_f32_16x16x32_bf16 v[8:11], a[120:123], a[24:27], v[8:11]\n"
    "ds_read_b64_tr_b16 a[172:173], v73 offset:19200\n"
    "ds_read_b64_tr_b16 a[174:175], v73 offset:27392\n"
    "v_mfma_f32_16x16x32_bf16 v[8:11], a[124:127], a[28:31], v[8:11]\n"
    "v_exp_f32 v22, v6\n"
    "v_mfma_f32_16x16x32_bf16 v[12:15], a[96:99], a[32:35], v[44:47]\n"
    "ds_read_b64_tr_b16 a[176:177], v73 offset:19456\n"
    "ds_read_b64_tr_b16 a[178:179], v73 offset:27648\n"
    "v_mfma_f32_16x16x32_bf16 v[12:15], a[100:103], a[36:39], v[12:15]\n"
    "v_exp_f32 v23, v7\n"
    "v_mfma_f32_16x16x32_bf16 v[12:15], a[104:107], a[40:43], v[12:15]\n"
    "ds_read_b64_tr_b16 a[180:181], v73 offset:19712\n"
    "ds_read_b64_tr_b16 a[182:183], v73 offset:27904\n"
    "v_mfma_f32_16x16x32_bf16 v[12:15], a[108:111], a[44:47], v[12:15]\n"
    "v_exp_f32 v24, v8\n"
    "v_mfma_f32_16x16x32_bf16 v[12:15], a[112:115], a[48:51], v[12:15]\n"
    "ds_read_b64_tr_b16 a[184:185], v73 offset:19968\n"
    "ds_read_b64_tr_b16 a[186:187], v73 offset:28160\n"
    "v_mfma_f32_16x16x32_bf16 v[12:15], a[116:119], a[52:55], v[12:15]\n"
    "v_exp_f32 v25, v9\n"
    "v_mfma_f32_16x16x32_bf16 v[12:15], a[120:123], a[56:59], v[12:15]\n"
    "ds_read_b64_tr_b16 a[188:189], v73 offset:20224\n"
    "ds_read_b64_tr_b16 a[190:191], v73 offset:28416\n"
    "v_mfma_f32_16x16x32_bf16 v[12:15], a[124:127], a[60:63], v[12:15]\n"
    "s_waitcnt lgkmcnt(8)\n"
    "v_exp_f32 v26, v10\n"
    "v_mfma_f32_16x16x32_bf16 v[128:131], a[128:131], v[56:59], v[128:131]\n"
    "ds_read_b128 a[64:67], v69 offset:49152\n"
    "v_add_f32 v64, v66, v16\n"
    "v_mfma_f32_16x16x32_bf16 v[132:135], a[128:131], v[60:63], v[132:135]\n"
    "v_exp_f32 v27, v11\n"
    "v_mfma_f32_16x16x32_bf16 v[136:139], a[132:135], v[56:59], v[136:139]\n"
    "ds_read_b128 a[68:71], v70 offset:49152\n"
    "v_add_f32 v64, v64, v17\n"
    "v_mfma_f32_16x16x32_bf16 v[140:143], a[132:135], v[60:63], v[140:143]\n"
    "v_exp_f32 v28, v12\n"
    "v_mfma_f32_16x16x32_bf16 v[144:147], a[136:139], v[56:59], v[144:147]\n"
    "ds_read_b128 a[72:75], v69 offset:50176\n"
    "v_add_f32 v64, v64, v18\n"
    "v_mfma_f32_16x16x32_bf16 v[148:151], a[136:139], v[60:63], v[148:151]\n"
    "v_exp_f32 v29, v13\n"
    "v_mfma_f32_16x16x32_bf16 v[152:155], a[140:143], v[56:59], v[152:155]\n"
    "ds_read_b128 a[76:79], v70 offset:50176\n"
    "v_add_f32 v64, v64, v19\n"
    "v_mfma_f32_16x16x32_bf16 v[156:159], a[140:143], v[60:63], v[156:159]\n"
    "v_exp_f32 v30, v14\n"
    "v_mfma_f32_16x16x32_bf16 v[160:163], a[144:147], v[56:59], v[160:163]\n"
    "ds_read_b128 a[80:83], v69 offset:51200\n"
    "v_add_f32 v65, v67, v20\n"
    "v_mfma_f32_16x16x32_bf16 v[164:167], a[144:147], v[60:63], v[164:167]\n"
    "v_exp_f32 v31, v15\n"
    "v_mfma_f32_16x16x32_bf16 v[168:171], a[148:151], v[56:59], v[168:171]\n"
    "s_add_u32 m0, s46, 0\n"
    "ds_read_b128 a[84:87], v70 offset:51200\n"
    "v_add_f32 v65, v65, v21\n"
    "v_mfma_f32_16x16x32_bf16 v[172:175], a[148:151], v[60:63], v[172:175]\n"
    "buffer_load_dwordx4 v79, s[28:31], s44 offen offset:0 nt lds\n"
    "v_mfma_f32_16x16x32_bf16 v[176:179], a[152:155], v[56:59], v[176:179]\n"
    "ds_read_b128 a[88:91], v69 offset:52224\n"
    "v_add_f32 v65, v65, v22\n"
    "v_mfma_f32_16x16x32_bf16 v[180:183], a[152:155], v[60:63], v[180:183]\n"
    "buffer_load_dwordx4 v80, s[28:31], s44 offen offset:1024 nt lds\n"
    "v_mfma_f32_16x16x32_bf16 v[184:187], a[156:159], v[56:59], v[184:187]\n"
    "ds_read_b128 a[92:95], v70 offset:52224\n"
    "v_add_f32 v65, v65, v23\n"
    "v_mfma_f32_16x16x32_bf16 v[188:191], a[156:159], v[60:63], v[188:191]\n"
    "buffer_load_dwordx4 v81, s[28:31], s44 offen offset:2048 nt lds\n"
    "v_mfma_f32_16x16x32_bf16 v[192:195], a[160:163], v[56:59], v[192:195]\n"
    "ds_read_b128 a[96:99], v69 offset:57344\n"
    "v_cvt_pk_bf16_f32 v48, v16, v17\n"
    "v_mfma_f32_16x16x32_bf16 v[196:199], a[160:163], v[60:63], v[196:199]\n"
    "buffer_load_dwordx4 v82, s[28:31], s44 offen offset:3072 nt lds\n"
    "v_mfma_f32_16x16x32_bf16 v[200:203], a[164:167], v[56:59], v[200:203]\n"
    "s_mov_b32 s44, s43\n"
    "ds_read_b128 a[100:103], v70 offset:57344\n"
    "v_cvt_pk_bf16_f32 v49, v18, v19\n"
    "v_mfma_f32_16x16x32_bf16 v[204:207], a[164:167], v[60:63], v[204:207]\n"
    "v_readlane_b32 s43, v83, s42\n"
    "v_mfma_f32_16x16x32_bf16 v[208:211], a[168:171], v[56:59], v[208:211]\n"
    "s_sub_u32 s35, s35, 1\n"
    "ds_read_b128 a[104:107], v69 offset:58368\n"
    "v_add_f32 v64, v64, v24\n"
    "v_mfma_f32_16x16x32_bf16 v[212:215], a[168:171], v[60:63], v[212:215]\n"
    "v_add_f32 v64, v64, v25\n"
    "v_add_f32 v64, v64, v26\n"
    "v_mfma_f32_16x16x32_bf16 v[216:219], a[172:175], v[56:59], v[216:219]\n"
    "s_cmp_le_u32 s35, 3\n"
    "ds_read_b128 a[108:111], v70 offset:58368\n"
    "v_add_f32 v64, v64, v27\n"
    "v_mfma_f32_16x16x32_bf16 v[220:223], a[172:175], v[60:63], v[220:223]\n"
    "s_waitcnt lgkmcnt(12)\n"
    "v_add_f32 v65, v65, v28\n"
    "v_add_f32 v65, v65, v29\n"
    "v_mfma_f32_16x16x32_bf16 v[224:227], a[176:179], v[56:59], v[224:227]\n"
    "s_cselect_b32 s99, s33, s47\n"
    "ds_read_b128 a[112:115], v69 offset:59392\n"
    "v_add_f32 v65, v65, v30\n"
    "v_mfma_f32_16x16x32_bf16 v[228:231], a[176:179], v[60:63], v[228:231]\n"
    "v_add_f32 v65, v65, v31\n"
    "v_cvt_pk_bf16_f32 v52, v20, v21\n"
    "v_mfma_f32_16x16x32_bf16 v[232:235], a[180:183], v[56:59], v[232:235]\n"
    "ds_read_b128 a[116:119], v70 offset:59392\n"
    "v_cvt_pk_bf16_f32 v53, v22, v23\n"
    "v_mfma_f32_16x16x32_bf16 v[236:239], a[180:183], v[60:63], v[236:239]\n"
    "v_cvt_pk_bf16_f32 v50, v24, v25\n"
    "v_cvt_pk_bf16_f32 v51, v26, v27\n"
    "v_mfma_f32_16x16x32_bf16 v[240:243], a[184:187], v[56:59], v[240:243]\n"
    "ds_read_b128 a[120:123], v69 offset:60416\n"
    "v_max_f32 v68, v64, v65\n"
    "v_mfma_f32_16x16x32_bf16 v[244:247], a[184:187], v[60:63], v[244:247]\n"
    "s_waitcnt vmcnt(12)\n"
    "s_barrier\n"
    "v_mfma_f32_16x16x32_bf16 v[248:251], a[188:191], v[56:59], v[248:251]\n"
    "ds_read_b128 a[124:127], v70 offset:60416\n"
    "v_cmp_nge_f32 vcc, s99, v68\n"
    "v_mfma_f32_16x16x32_bf16 v[252:255], a[188:191], v[60:63], v[252:255]\n"
    "s_waitcnt lgkmcnt(8)\n"
    "s_cbranch_vccnz .Lstub1\n"
    ".Ltop2:\n"
    "v_mfma_f32_16x16x32_bf16 v[0:3], a[64:67], a[0:3], v[32:35]\n"
    "s_add_u32 m0, s45, 32768\n"
    "ds_read_b64_tr_b16 a[128:129], v73 offset:32768\n"
    "ds_read_b64_tr_b16 a[130:131], v73 offset:40960\n"
    "v_mfma_f32_16x16x32_bf16 v[0:3], a[68:71], a[4:7], v[0:3]\n"
    "buffer_load_dwordx4 v75, s[24:27], s43 offen offset:0 nt lds\n"
    "v_mfma_f32_16x16x32_bf16 v[0:3], a[72:75], a[8:11], v[0:3]\n"
    "ds_read_b64_tr_b16 a[132:133], v73 offset:33024\n"
    "ds_read_b64_tr_b16 a[134:135], v73 offset:41216\n"
    "v_mfma_f32_16x16x32_bf16 v[0:3], a[76:79], a[12:15], v[0:3]\n"
    "buffer_load_dwordx4 v76, s[24:27], s43 offen offset:1024 nt lds\n"
    "v_mfma_f32_16x16x32_bf16 v[0:3], a[80:83], a[16:19], v[0:3]\n"
    "ds_read_b64_tr_b16 a[136:137], v73 offset:33280\n"
    "ds_read_b64_tr_b16 a[138:139], v73 offset:41472\n"
    "v_mfma_f32_16x16x32_bf16 v[0:3], a[84:87], a[20:23], v[0:3]\n"
    "buffer_load_dwordx4 v77, s[24:27], s43 offen offset:2048 nt lds\n"
    "v_mfma_f32_16x16x32_bf16 v[0:3], a[88:91], a[24:27], v[0:3]\n"
    "ds_read_b64_tr_b16 a[140:141], v73 offset:33536\n"
    "ds_read_b64_tr_b16 a[142:143], v73 offset:41728\n"
    "v_mfma_f32_16x16x32_bf16 v[0:3], a[92:95], a[28:31], v[0:3]\n"
    "buffer_load_dwordx4 v78, s[24:27], s43 offen offset:3072 nt lds\n"
    "v_mfma_f32_16x16x32_bf16 v[4:7], a[64:67], a[32:35], v[36:39]\n"
    "s_add_u32 s42, s42, 1\n"
    "ds_read_b64_tr_b16 a[144:145], v73 offset:33792\n"
    "ds_read_b64_tr_b16 a[146:147], v73 offset:41984\n"
    "v_mfma_f32_16x16x32_bf16 v[4:7], a[68:71], a[36:39], v[4:7]\n"
    "v_cvt_pk_bf16_f32 v54, v28, v29\n"
    "v_cvt_pk_bf16_f32 v55, v30, v31\n"
    "v_mfma_f32_16x16x32_bf16 v[4:7], a[72:75], a[40:43], v[4:7]\n"
    "ds_read_b64_tr_b16 a[148:149], v73 offset:34048\n"
    "ds_read_b64_tr_b16 a[150:151], v73 offset:42240\n"
    "v_mfma_f32_16x16x32_bf16 v[4:7], a[76:79], a[44:47], v[4:7]\n"
    "v_exp_f32 v16, v0\n"
    "v_mfma_f32_16x16x32_bf16 v[4:7], a[80:83], a[48:51], v[4:7]\n"
    "ds_read_b64_tr_b16 a[152:153], v73 offset:34304\n"
    "ds_read_b64_tr_b16 a[154:155], v73 offset:42496\n"
    "v_mfma_f32_16x16x32_bf16 v[4:7], a[84:87], a[52:55], v[4:7]\n"
    "v_exp_f32 v17, v1\n"
    "v_mfma_f32_16x16x32_bf16 v[4:7], a[88:91], a[56:59], v[4:7]\n"
    "ds_read_b64_tr_b16 a[156:157], v73 offset:34560\n"
    "ds_read_b64_tr_b16 a[158:159], v73 offset:42752\n"
    "v_mfma_f32_16x16x32_bf16 v[4:7], a[92:95], a[60:63], v[4:7]\n"
    "s_waitcnt lgkmcnt(12)\n"
    "v_exp_f32 v18, v2\n"
    "v_mfma_f32_16x16x32_bf16 v[8:11], a[96:99], a[0:3], v[40:43]\n"
    "ds_read_b64_tr_b16 a[160:161], v73 offset:34816\n"
    "ds_read_b64_tr_b16 a[162:163], v73 offset:43008\n"
    "v_mfma_f32_16x16x32_bf16 v[8:11], a[100:103], a[4:7], v[8:11]\n"
    "v_exp_f32 v19, v3\n"
    "v_mfma_f32_16x16x32_bf16 v[8:11], a[104:107], a[8:11], v[8:11]\n"
    "ds_read_b64_tr_b16 a[164:165], v73 offset:35072\n"
    "ds_read_b64_tr_b16 a[166:167], v73 offset:43264\n"
    "v_mfma_f32_16x16x32_bf16 v[8:11], a[108:111], a[12:15], v[8:11]\n"
    "v_exp_f32 v20, v4\n"
    "v_mfma_f32_16x16x32_bf16 v[8:11], a[112:115], a[16:19], v[8:11]\n"
    "ds_read_b64_tr_b16 a[168:169], v73 offset:35328\n"
    "ds_read_b64_tr_b16 a[170:171], v73 offset:43520\n"
    "v_mfma_f32_16x16x32_bf16 v[8:11], a[116:119], a[20:23], v[8:11]\n"
    "v_exp_f32 v21, v5\n"
    "v_mfma_f32_16x16x32_bf16 v[8:11], a[120:123], a[24:27], v[8:11]\n"
    "ds_read_b64_tr_b16 a[172:173], v73 offset:35584\n"
    "ds_read_b64_tr_b16 a[174:175], v73 offset:43776\n"
    "v_mfma_f32_16x16x32_bf16 v[8:11], a[124:127], a[28:31], v[8:11]\n"
    "v_exp_f32 v22, v6\n"
    "v_mfma_f32_16x16x32_bf16 v[12:15], a[96:99], a[32:35], v[44:47]\n"
    "ds_read_b64_tr_b16 a[176:177], v73 offset:35840\n"
    "ds_read_b64_tr_b16 a[178:179], v73 offset:44032\n"
    "v_mfma_f32_16x16x32_bf16 v[12:15], a[100:103], a[36:39], v[12:15]\n"
    "v_exp_f32 v23, v7\n"
    "v_mfma_f32_16x16x32_bf16 v[12:15], a[104:107], a[40:43], v[12:15]\n"
    "ds_read_b64_tr_b16 a[180:181], v73 offset:36096\n"
    "ds_read_b64_tr_b16 a[182:183], v73 offset:44288\n"
    "v_mfma_f32_16x16x32_bf16 v[12:15], a[108:111], a[44:47], v[12:15]\n"
    "v_exp_f32 v24, v8\n"
    "v_mfma_f32_16x16x32_bf16 v[12:15], a[112:115], a[48:51], v[12:15]\n"
    "ds_read_b64_tr_b16 a[184:185], v73 offset:36352\n"
    "ds_read_b64_tr_b16 a[186:187], v73 offset:44544\n"
    "v_mfma_f32_16x16x32_bf16 v[12:15], a[116:119], a[52:55], v[12:15]\n"
    "v_exp_f32 v25, v9\n"
    "v_mfma_f32_16x16x32_bf16 v[12:15], a[120:123], a[56:59], v[12:15]\n"
    "ds_read_b64_tr_b16 a[188:189], v73 offset:36608\n"
    "ds_read_b64_tr_b16 a[190:191], v73 offset:44800\n"
    "v_mfma_f32_16x16x32_bf16 v[12:15], a[124:127], a[60:63], v[12:15]\n"
    "s_waitcnt lgkmcnt(8)\n"
    "v_exp_f32 v26, v10\n"
    "v_mfma_f32_16x16x32_bf16 v[128:131], a[128:131], v[48:51], v[128:131]\n"
    "ds_read_b128 a[64:67], v69 offset:0\n"
    "v_add_f32 v66, v64, v16\n"
    "v_mfma_f32_16x16x32_bf16 v[132:135], a[128:131], v[52:55], v[132:135]\n"
    "v_exp_f32 v27, v11\n"
    "v_mfma_f32_16x16x32_bf16 v[136:139], a[132:135], v[48:51], v[136:139]\n"
    "ds_read_b128 a[68:71], v70 offset:0\n"
    "v_add_f32 v66, v66, v17\n"
    "v_mfma_f32_16x16x32_bf16 v[140:143], a[132:135], v[52:55], v[140:143]\n"
    "v_exp_f32 v28, v12\n"
    "v_mfma_f32_16x16x32_bf16 v[144:147], a[136:139], v[48:51], v[144:147]\n"
    "ds_read_b128 a[72:75], v69 offset:1024\n"
    "v_add_f32 v66, v66, v18\n"
    "v_mfma_f32_16x16x32_bf16 v[148:151], a[136:139], v[52:55], v[148:151]\n"
    "v_exp_f32 v29, v13\n"
    "v_mfma_f32_16x16x32_bf16 v[152:155], a[140:143], v[48:51], v[152:155]\n"
    "ds_read_b128 a[76:79], v70 offset:1024\n"
    "v_add_f32 v66, v66, v19\n"
    "v_mfma_f32_16x16x32_bf16 v[156:159], a[140:143], v[52:55], v[156:159]\n"
    "v_exp_f32 v30, v14\n"
    "v_mfma_f32_16x16x32_bf16 v[160:163], a[144:147], v[48:51], v[160:163]\n"
    "ds_read_b128 a[80:83], v69 offset:2048\n"
    "v_add_f32 v67, v65, v20\n"
    "v_mfma_f32_16x16x32_bf16 v[164:167], a[144:147], v[52:55], v[164:167]\n"
    "v_exp_f32 v31, v15\n"
    "v_mfma_f32_16x16x32_bf16 v[168:171], a[148:151], v[48:51], v[168:171]\n"
    "s_add_u32 m0, s46, 16384\n"
    "ds_read_b128 a[84:87], v70 offset:2048\n"
    "v_add_f32 v67, v67, v21\n"
    "v_mfma_f32_16x16x32_bf16 v[172:175], a[148:151], v[52:55], v[172:175]\n"
    "buffer_load_dwordx4 v79, s[28:31], s44 offen offset:0 nt lds\n"
    "v_mfma_f32_16x16x32_bf16 v[176:179], a[152:155], v[48:51], v[176:179]\n"
    "ds_read_b128 a[88:91], v69 offset:3072\n"
    "v_add_f32 v67, v67, v22\n"
    "v_mfma_f32_16x16x32_bf16 v[180:183], a[152:155], v[52:55], v[180:183]\n"
    "buffer_load_dwordx4 v80, s[28:31], s44 offen offset:1024 nt lds\n"
    "v_mfma_f32_16x16x32_bf16 v[184:187], a[156:159], v[48:51], v[184:187]\n"
    "ds_read_b128 a[92:95], v70 offset:3072\n"
    "v_add_f32 v67, v67, v23\n"
    "v_mfma_f32_16x16x32_bf16 v[188:191], a[156:159], v[52:55], v[188:191]\n"
    "buffer_load_dwordx4 v81, s[28:31], s44 offen offset:2048 nt lds\n"
    "v_mfma_f32_16x16x32_bf16 v[192:195], a[160:163], v[48:51], v[192:195]\n"
    "ds_read_b128 a[96:99], v69 offset:8192\n"
    "v_cvt_pk_bf16_f32 v56, v16, v17\n"
    "v_mfma_f32_16x16x32_bf16 v[196:199], a[160:163], v[52:55], v[196:199]\n"
    "buffer_load_dwordx4 v82, s[28:31], s44 offen offset:3072 nt lds\n"
    "v_mfma_f32_16x16x32_bf16 v[200:203], a[164:167], v[48:51], v[200:203]\n"
    "s_mov_b32 s44, s43\n"
    "ds_read_b128 a[100:103], v70 offset:8192\n"
    "v_cvt_pk_bf16_f32 v57, v18, v19\n"
    "v_mfma_f32_16x16x32_bf16 v[204:207], a[164:167], v[52:55], v[204:207]\n"
    "v_readlane_b32 s43, v83, s42\n"
    "v_mfma_f32_16x16x32_bf16 v[208:211], a[168:171], v[48:51], v[208:211]\n"
    "s_sub_u32 s35, s35, 1\n"
    "ds_read_b128 a[104:107], v69 offset:9216\n"
    "v_add_f32 v66, v66, v24\n"
    "v_mfma_f32_16x16x32_bf16 v[212:215], a[168:171], v[52:55], v[212:215]\n"
    "v_add_f32 v66, v66, v25\n"
    "v_add_f32 v66, v66, v26\n"
    "v_mfma_f32_16x16x32_bf16 v[216:219], a[172:175], v[48:51], v[216:219]\n"
    "s_cmp_le_u32 s35, 3\n"
    "ds_read_b128 a[108:111], v70 offset:9216\n"
    "v_add_f32 v66, v66, v27\n"
    "v_mfma_f32_16x16x32_bf16 v[220:223], a[172:175], v[52:55], v[220:223]\n"
    "s_waitcnt lgkmcnt(12)\n"
    "v_add_f32 v67, v67, v28\n"
    "v_add_f32 v67, v67, v29\n"
    "v_mfma_f32_16x16x32_bf16 v[224:227], a[176:179], v[48:51], v[224:227]\n"
    "s_cselect_b32 s99, s33, s47\n"
    "ds_read_b128 a[112:115], v69 offset:10240\n"
    "v_add_f32 v67, v67, v30\n"
    "v_mfma_f32_16x16x32_bf16 v[228:231], a[176:179], v[52:55], v[228:231]\n"
    "v_add_f32 v67, v67, v31\n"
    "v_cvt_pk_bf16_f32 v60, v20, v21\n"
    "v_mfma_f32_16x16x32_bf16 v[232:235], a[180:183], v[48:51], v[232:235]\n"
    "ds_read_b128 a[116:119], v70 offset:10240\n"
    "v_cvt_pk_bf16_f32 v61, v22, v23\n"
    "v_mfma_f32_16x16x32_bf16 v[236:239], a[180:183], v[52:55], v[236:239]\n"
    "v_cvt_pk_bf16_f32 v58, v24, v25\n"
    "v_cvt_pk_bf16_f32 v59, v26, v27\n"
    "v_mfma_f32_16x16x32_bf16 v[240:243], a[184:187], v[48:51], v[240:243]\n"
    "ds_read_b128 a[120:123], v69 offset:11264\n"
    "v_max_f32 v68, v66, v67\n"
    "v_mfma_f32_16x16x32_bf16 v[244:247], a[184:187], v[52:55], v[244:247]\n"
    "s_waitcnt vmcnt(12)\n"
    "s_barrier\n"
    "v_mfma_f32_16x16x32_bf16 v[248:251], a[188:191], v[48:51], v[248:251]\n"
    "ds_read_b128 a[124:127], v70 offset:11264\n"
    "v_cmp_nge_f32 vcc, s99, v68\n"
    "v_mfma_f32_16x16x32_bf16 v[252:255], a[188:191], v[52:55], v[252:255]\n"
    "s_waitcnt lgkmcnt(8)\n"
    "s_cbranch_vccnz .Lstub2\n"
    ".Ltop3:\n"
    "v_mfma_f32_16x16x32_bf16 v[0:3], a[64:67], a[0:3], v[32:35]\n"
    "s_add_u32 m0, s45, 49152\n"
    "ds_read_b64_tr_b16 a[128:129], v73 offset:49152\n"
    "ds_read_b64_tr_b16 a[130:131], v73 offset:57344\n"
    "v_mfma_f32_16x16x32_bf16 v[0:3], a[68:71], a[4:7], v[0:3]\n"
    "buffer_load_dwordx4 v75, s[24:27], s43 offen offset:0 nt lds\n"
    "v_mfma_f32_16x16x32_bf16 v[0:3], a[72:75], a[8:11], v[0:3]\n"
    "ds_read_b64_tr_b16 a[132:133], v73 offset:49408\n"
    "ds_read_b64_tr_b16 a[134:135], v73 offset:57600\n"
    "v_mfma_f32_16x16x32_bf16 v[0:3], a[76:79], a[12:15], v[0:3]\n"
    "buffer_load_dwordx4 v76, s[24:27], s43 offen offset:1024 nt lds\n"
    "v_mfma_f32_16x16x32_bf16 v[0:3], a[80:83], a[16:19], v[0:3]\n"
    "ds_read_b64_tr_b16 a[136:137], v73 offset:49664\n"
    "ds_read_b64_tr_b16 a[138:139], v73 offset:57856\n"
    "v_mfma_f32_16x16x32_bf16 v[0:3], a[84:87], a[20:23], v[0:3]\n"
    "buffer_load_dwordx4 v77, s[24:27], s43 offen offset:2048 nt lds\n"
    "v_mfma_f32_16x16x32_bf16 v[0:3], a[88:91], a[24:27], v[0:3]\n"
    "ds_read_b64_tr_b16 a[140:141], v73 offset:49920\n"
    "ds_read_b64_tr_b16 a[142:143], v73 offset:58112\n"
    "v_mfma_f32_16x16x32_bf16 v[0:3], a[92:95], a[28:31], v[0:3]\n"
    "buffer_load_dwordx4 v78, s[24:27], s43 offen offset:3072 nt lds\n"
    "v_mfma_f32_16x16x32_bf16 v[4:7], a[64:67], a[32:35], v[36:39]\n"
    "s_add_u32 s42, s42, 1\n"
    "ds_read_b64_tr_b16 a[144:145], v73 offset:50176\n"
    "ds_read_b64_tr_b16 a[146:147], v73 offset:58368\n"
    "v_mfma_f32_16x16x32_bf16 v[4:7], a[68:71], a[36:39], v[4:7]\n"
    "v_cvt_pk_bf16_f32 v62, v28, v29\n"
    "v_cvt_pk_bf16_f32 v63, v30, v31\n"
    "v_mfma_f32_16x16x32_bf16 v[4:7], a[72:75], a[40:43], v[4:7]\n"
    "ds_read_b64_tr_b16 a[148:149], v73 offset:50432\n"
    "ds_read_b64_tr_b16 a[150:151], v73 offset:58624\n"
    "v_mfma_f32_16x16x32_bf16 v[4:7], a[76:79], a[44:47], v[4:7]\n"
    "v_exp_f32 v16, v0\n"
    "v_mfma_f32_16x16x32_bf16 v[4:7], a[80:83], a[48:51], v[4:7]\n"
    "ds_read_b64_tr_b16 a[152:153], v73 offset:50688\n"
    "ds_read_b64_tr_b16 a[154:155], v73 offset:58880\n"
    "v_mfma_f32_16x16x32_bf16 v[4:7], a[84:87], a[52:55], v[4:7]\n"
    "v_exp_f32 v17, v1\n"
    "v_mfma_f32_16x16x32_bf16 v[4:7], a[88:91], a[56:59], v[4:7]\n"
    "ds_read_b64_tr_b16 a[156:157], v73 offset:50944\n"
    "ds_read_b64_tr_b16 a[158:159], v73 offset:59136\n"
    "v_mfma_f32_16x16x32_bf16 v[4:7], a[92:95], a[60:63], v[4:7]\n"
    "s_waitcnt lgkmcnt(12)\n"
    "v_exp_f32 v18, v2\n"
    "v_mfma_f32_16x16x32_bf16 v[8:11], a[96:99], a[0:3], v[40:43]\n"
    "ds_read_b64_tr_b16 a[160:161], v73 offset:51200\n"
    "ds_read_b64_tr_b16 a[162:163], v73 offset:59392\n"
    "v_mfma_f32_16x16x32_bf16 v[8:11], a[100:103], a[4:7], v[8:11]\n"
    "v_exp_f32 v19, v3\n"
    "v_mfma_f32_16x16x32_bf16 v[8:11], a[104:107], a[8:11], v[8:11]\n"
    "ds_read_b64_tr_b16 a[164:165], v73 offset:51456\n"
    "ds_read_b64_tr_b16 a[166:167], v73 offset:59648\n"
    "v_mfma_f32_16x16x32_bf16 v[8:11], a[108:111], a[12:15], v[8:11]\n"
    "v_exp_f32 v20, v4\n"
    "v_mfma_f32_16x16x32_bf16 v[8:11], a[112:115], a[16:19], v[8:11]\n"
    "ds_read_b64_tr_b16 a[168:169], v73 offset:51712\n"
    "ds_read_b64_tr_b16 a[170:171], v73 offset:59904\n"
    "v_mfma_f32_16x16x32_bf16 v[8:11], a[116:119], a[20:23], v[8:11]\n"
    "v_exp_f32 v21, v5\n"
    "v_mfma_f32_16x16x32_bf16 v[8:11], a[120:123], a[24:27], v[8:11]\n"
    "ds_read_b64_tr_b16 a[172:173], v73 offset:51968\n"
    "ds_read_b64_tr_b16 a[174:175], v73 offset:60160\n"
    "v_mfma_f32_16x16x32_bf16 v[8:11], a[124:127], a[28:31], v[8:11]\n"
    "v_exp_f32 v22, v6\n"
    "v_mfma_f32_16x16x32_bf16 v[12:15], a[96:99], a[32:35], v[44:47]\n"
    "ds_read_b64_tr_b16 a[176:177], v73 offset:52224\n"
    "ds_read_b64_tr_b16 a[178:179], v73 offset:60416\n"
    "v_mfma_f32_16x16x32_bf16 v[12:15], a[100:103], a[36:39], v[12:15]\n"
    "v_exp_f32 v23, v7\n"
    "v_mfma_f32_16x16x32_bf16 v[12:15], a[104:107], a[40:43], v[12:15]\n"
    "ds_read_b64_tr_b16 a[180:181], v73 offset:52480\n"
    "ds_read_b64_tr_b16 a[182:183], v73 offset:60672\n"
    "v_mfma_f32_16x16x32_bf16 v[12:15], a[108:111], a[44:47], v[12:15]\n"
    "v_exp_f32 v24, v8\n"
    "v_mfma_f32_16x16x32_bf16 v[12:15], a[112:115], a[48:51], v[12:15]\n"
    "ds_read_b64_tr_b16 a[184:185], v73 offset:52736\n"
    "ds_read_b64_tr_b16 a[186:187], v73 offset:60928\n"
    "v_mfma_f32_16x16x32_bf16 v[12:15], a[116:119], a[52:55], v[12:15]\n"
    "v_exp_f32 v25, v9\n"
    "v_mfma_f32_16x16x32_bf16 v[12:15], a[120:123], a[56:59], v[12:15]\n"
    "ds_read_b64_tr_b16 a[188:189], v73 offset:52992\n"
    "ds_read_b64_tr_b16 a[190:191], v73 offset:61184\n"
    "v_mfma_f32_16x16x32_bf16 v[12:15], a[124:127], a[60:63], v[12:15]\n"
    "s_waitcnt lgkmcnt(8)\n"
    "v_exp_f32 v26, v10\n"
    "v_mfma_f32_16x16x32_bf16 v[128:131], a[128:131], v[56:59], v[128:131]\n"
    "ds_read_b128 a[64:67], v69 offset:16384\n"
    "v_add_f32 v64, v66, v16\n"
    "v_mfma_f32_16x16x32_bf16 v[132:135], a[128:131], v[60:63], v[132:135]\n"
    "v_exp_f32 v27, v11\n"
    "v_mfma_f32_16x16x32_bf16 v[136:139], a[132:135], v[56:59], v[136:139]\n"
    "ds_read_b128 a[68:71], v70 offset:16384\n"
    "v_add_f32 v64, v64, v17\n"
    "v_mfma_f32_16x16x32_bf16 v[140:143], a[132:135], v[60:63], v[140:143]\n"
    "v_exp_f32 v28, v12\n"
    "v_mfma_f32_16x16x32_bf16 v[144:147], a[136:139], v[56:59], v[144:147]\n"
    "ds_read_b128 a[72:75], v69 offset:17408\n"
    "v_add_f32 v64, v64, v18\n"
    "v_mfma_f32_16x16x32_bf16 v[148:151], a[136:139], v[60:63], v[148:151]\n"
    "v_exp_f32 v29, v13\n"
    "v_mfma_f32_16x16x32_bf16 v[152:155], a[140:143], v[56:59], v[152:155]\n"
    "ds_read_b128 a[76:79], v70 offset:17408\n"
    "v_add_f32 v64, v64, v19\n"
    "v_mfma_f32_16x16x32_bf16 v[156:159], a[140:143], v[60:63], v[156:159]\n"
    "v_exp_f32 v30, v14\n"
    "v_mfma_f32_16x16x32_bf16 v[160:163], a[144:147], v[56:59], v[160:163]\n"
    "ds_read_b128 a[80:83], v69 offset:18432\n"
    "v_add_f32 v65, v67, v20\n"
    "v_mfma_f32_16x16x32_bf16 v[164:167], a[144:147], v[60:63], v[164:167]\n"
    "v_exp_f32 v31, v15\n"
    "v_mfma_f32_16x16x32_bf16 v[168:171], a[148:151], v[56:59], v[168:171]\n"
    "s_add_u32 m0, s46, 32768\n"
    "ds_read_b128 a[84:87], v70 offset:18432\n"
    "v_add_f32 v65, v65, v21\n"
    "v_mfma_f32_16x16x32_bf16 v[172:175], a[148:151], v[60:63], v[172:175]\n"
    "buffer_load_dwordx4 v79, s[28:31], s44 offen offset:0 nt lds\n"
    "v_mfma_f32_16x16x32_bf16 v[176:179], a[152:155], v[56:59], v[176:179]\n"
    "ds_read_b128 a[88:91], v69 offset:19456\n"
    "v_add_f32 v65, v65, v22\n"
    "v_mfma_f32_16x16x32_bf16 v[180:183], a[152:155], v[60:63], v[180:183]\n"
    "buffer_load_dwordx4 v80, s[28:31], s44 offen offset:1024 nt lds\n"
    "v_mfma_f32_16x16x32_bf16 v[184:187], a[156:159], v[56:59], v[184:187]\n"
    "ds_read_b128 a[92:95], v70 offset:19456\n"
    "v_add_f32 v65, v65, v23\n"
    "v_mfma_f32_16x16x32_bf16 v[188:191], a[156:159], v[60:63], v[188:191]\n"
    "buffer_load_dwordx4 v81, s[28:31], s44 offen offset:2048 nt lds\n"
    "v_mfma_f32_16x16x32_bf16 v[192:195], a[160:163], v[56:59], v[192:195]\n"
    "ds_read_b128 a[96:99], v69 offset:24576\n"
    "v_cvt_pk_bf16_f32 v48, v16, v17\n"
    "v_mfma_f32_16x16x32_bf16 v[196:199], a[160:163], v[60:63], v[196:199]\n"
    "buffer_load_dwordx4 v82, s[28:31], s44 offen offset:3072 nt lds\n"
    "v_mfma_f32_16x16x32_bf16 v[200:203], a[164:167], v[56:59], v[200:203]\n"
    "s_mov_b32 s44, s43\n"
    "ds_read_b128 a[100:103], v70 offset:24576\n"
    "v_cvt_pk_bf16_f32 v49, v18, v19\n"
    "v_mfma_f32_16x16x32_bf16 v[204:207], a[164:167], v[60:63], v[204:207]\n"
    "v_readlane_b32 s43, v83, s42\n"
    "v_mfma_f32_16x16x32_bf16 v[208:211], a[168:171], v[56:59], v[208:211]\n"
    "s_sub_u32 s35, s35, 1\n"
    "ds_read_b128 a[104:107], v69 offset:25600\n"
    "v_add_f32 v64, v64, v24\n"
    "v_mfma_f32_16x16x32_bf16 v[212:215], a[168:171], v[60:63], v[212:215]\n"
    "v_add_f32 v64, v64, v25\n"
    "v_add_f32 v64, v64, v26\n"
    "v_mfma_f32_16x16x32_bf16 v[216:219], a[172:175], v[56:59], v[216:219]\n"
    "s_cmp_le_u32 s35, 3\n"
    "ds_read_b128 a[108:111], v70 offset:25600\n"
    "v_add_f32 v64, v64, v27\n"
    "v_mfma_f32_16x16x32_bf16 v[220:223], a[172:175], v[60:63], v[220:223]\n"
    "s_waitcnt lgkmcnt(12)\n"
    "v_add_f32 v65, v65, v28\n"
    "v_add_f32 v65, v65, v29\n"
    "v_mfma_f32_16x16x32_bf16 v[224:227], a[176:179], v[56:59], v[224:227]\n"
    "s_cselect_b32 s99, s33, s47\n"
    "ds_read_b128 a[112:115], v69 offset:26624\n"
    "v_add_f32 v65, v65, v30\n"
    "v_mfma_f32_16x16x32_bf16 v[228:231], a[176:179], v[60:63], v[228:231]\n"
    "v_add_f32 v65, v65, v31\n"
    "v_cvt_pk_bf16_f32 v52, v20, v21\n"
    "v_mfma_f32_16x16x32_bf16 v[232:235], a[180:183], v[56:59], v[232:235]\n"
    "ds_read_b128 a[116:119], v70 offset:26624\n"
    "v_cvt_pk_bf16_f32 v53, v22, v23\n"
    "v_mfma_f32_16x16x32_bf16 v[236:239], a[180:183], v[60:63], v[236:239]\n"
    "v_cvt_pk_bf16_f32 v50, v24, v25\n"
    "v_cvt_pk_bf16_f32 v51, v26, v27\n"
    "v_mfma_f32_16x16x32_bf16 v[240:243], a[184:187], v[56:59], v[240:243]\n"
    "ds_read_b128 a[120:123], v69 offset:27648\n"
    "v_max_f32 v68, v64, v65\n"
    "v_mfma_f32_16x16x32_bf16 v[244:247], a[184:187], v[60:63], v[244:247]\n"
    "s_waitcnt vmcnt(12)\n"
    "s_barrier\n"
    "v_mfma_f32_16x16x32_bf16 v[248:251], a[188:191], v[56:59], v[248:251]\n"
    "ds_read_b128 a[124:127], v70 offset:27648\n"
    "v_cmp_nge_f32 vcc, s99, v68\n"
    "v_mfma_f32_16x16x32_bf16 v[252:255], a[188:191], v[60:63], v[252:255]\n"
    "s_waitcnt lgkmcnt(8)\n"
    "s_cbranch_vccz .Ltop0\n"
    ".Lpf2:\n"
    ".Lstub3:\n"
    "v_cmp_nge_f32 vcc, s47, v68\n"
    "s_nop 4\n"
    "s_cbranch_vccz .Lstub3_noslow\n"
    "s_swappc_b64 s[56:57], s[50:51]\n"
    ".Lstub3_noslow:\n"
    "s_cmp_eq_u32 s35, 0\n"
    "s_cbranch_scc1 .Ltail3\n"
    "s_cmp_gt_u32 s35, 3\n"
    "s_cselect_b32 s26, s48, 0\n"
    "s_cmp_gt_u32 s35, 2\n"
    "s_cselect_b32 s30, s48, 0\n"
    "s_cmp_lg_u32 s35, 1\n"
    "s_cbranch_scc1 .Ltop0\n"
    "s_swappc_b64 s[56:57], s[54:55]\n"
    "s_branch .Ltop0\n"
    ".Lstub0:\n"
    "v_cmp_nge_f32 vcc, s47, v68\n"
    "s_nop 4\n"
    "s_cbranch_vccz .Lstub0_noslow\n"
    "s_swappc_b64 s[56:57], s[52:53]\n"
    ".Lstub0_noslow:\n"
    "s_cmp_eq_u32 s35, 0\n"
    "s_cbranch_scc1 .Ltail0\n"
    "s_cmp_gt_u32 s35, 3\n"
    "s_cselect_b32 s26, s48, 0\n"
    "s_cmp_gt_u32 s35, 2\n"
    "s_cselect_b32 s30, s48, 0\n"
    "s_cmp_lg_u32 s35, 1\n"
    "s_cbranch_scc1 .Ltop1\n"
    "s_swappc_b64 s[56:57], s[54:55]\n"
    "s_branch .Ltop1\n"
    ".Lstub1:\n"
    "v_cmp_nge_f32 vcc, s47, v68\n"
    "s_nop 4\n"
    "s_cbranch_vccz .Lstub1_noslow\n"
    "s_swappc_b64 s[56:57], s[50:51]\n"
    ".Lstub1_noslow:\n"
    "s_cmp_eq_u32 s35, 0\n"
    "s_cbranch_scc1 .Ltail1\n"
    "s_cmp_gt_u32 s35, 3\n"
    "s_cselect_b32 s26, s48, 0\n"
    "s_cmp_gt_u32 s35, 2\n"
    "s_cselect_b32 s30, s48, 0\n"
    "s_cmp_lg_u32 s35, 1\n"
    "s_cbranch_scc1 .Ltop2\n"
    "s_swappc_b64 s[56:57], s[54:55]\n"
    "s_branch .Ltop2\n"
    ".Lstub2:\n"
    "v_cmp_nge_f32 vcc, s47, v68\n"
    "s_nop 4\n"
    "s_cbranch_vccz .Lstub2_noslow\n"
    "s_swappc_b64 s[56:57], s[52:53]\n"
    ".Lstub2_noslow:\n"
    "s_cmp_eq_u32 s35, 0\n"
    "s_cbranch_scc1 .Ltail2\n"
    "s_cmp_gt_u32 s35, 3\n"
    "s_cselect_b32 s26, s48, 0\n"
    "s_cmp_gt_u32 s35, 2\n"
    "s_cselect_b32 s30, s48, 0\n"
    "s_cmp_lg_u32 s35, 1\n"
    "s_cbranch_scc1 .Ltop3\n"
    "s_swappc_b64 s[56:57], s[54:55]\n"
    "s_branch .Ltop3\n"
    ".Lmask:\n"
    "v_mbcnt_lo_u32_b32 v120, -1, 0\n"
    "v_mbcnt_hi_u32_b32 v120, -1, v120\n"
    "v_lshrrev_b32 v120, 4, v120\n"
    "v_lshlrev_b32 v120, 2, v120\n"
    "s_sub_u32 s49, s34, 1\n"
    "s_lshl_b32 s49, s49, 5\n"
    "s_sub_u32 s49, s40, s49\n"
    "v_mov_b32 v121, s68\n"
    "v_add_u32 v122, 0, v120\n"
    "v_cmp_gt_u32 vcc, s49, v122\n"
    "s_nop 1\n"
    "v_cndmask_b32 v32, v121, v32, vcc\n"
    "v_cndmask_b32 v36, v121, v36, vcc\n"
    "v_add_u32 v122, 1, v120\n"
    "v_cmp_gt_u32 vcc, s49, v122\n"
    "s_nop 1\n"
    "v_cndmask_b32 v33, v121, v33, vcc\n"
    "v_cndmask_b32 v37, v121, v37, vcc\n"
    "v_add_u32 v122, 2, v120\n"
    "v_cmp_gt_u32 vcc, s49, v122\n"
    "s_nop 1\n"
    "v_cndmask_b32 v34, v121, v34, vcc\n"
    "v_cndmask_b32 v38, v121, v38, vcc\n"
    "v_add_u32 v122, 3, v120\n"
    "v_cmp_gt_u32 vcc, s49, v122\n"
    "s_nop 1\n"
    "v_cndmask_b32 v35, v121, v35, vcc\n"
    "v_cndmask_b32 v39, v121, v39, vcc\n"
    "v_add_u32 v122, 16, v120\n"
    "v_cmp_gt_u32 vcc, s49, v122\n"
    "s_nop 1\n"
    "v_cndmask_b32 v40, v121, v40, vcc\n"
    "v_cndmask_b32 v44, v121, v44, vcc\n"
    "v_add_u32 v122, 17, v120\n"
    "v_cmp_gt_u32 vcc, s49, v122\n"
    "s_nop 1\n"
    "v_cndmask_b32 v41, v121, v41, vcc\n"
    "v_cndmask_b32 v45, v121, v45, vcc\n"
    "v_add_u32 v122, 18, v120\n"
    "v_cmp_gt_u32 vcc, s49, v122\n"
    "s_nop 1\n"
    "v_cndmask_b32 v42, v121, v42, vcc\n"
    "v_cndmask_b32 v46, v121, v46, vcc\n"
    "v_add_u32 v122, 19, v120\n"
    "v_cmp_gt_u32 vcc, s49, v122\n"
    "s_nop 1\n"
    "v_cndmask_b32 v43, v121, v43, vcc\n"
    "v_cndmask_b32 v47, v121, v47, vcc\n"
    "s_mov_b64 vcc, 0\n"
    "s_nop 3\n"
    "s_setpc_b64 s[56:57]\n"
    ".Ltail0:\n"
    "v_add_u32 v84, 16384, v73\n"
    "v_cvt_pk_bf16_f32 v62, v28, v29\n"
    "v_cvt_pk_bf16_f32 v63, v30, v31\n"
    "s_nop 0\n"
    "v_mov_b32 v48, v56\n"
    "v_mov_b32 v49, v57\n"
    "v_mov_b32 v50, v58\n"
    "v_mov_b32 v51, v59\n"
    "v_mov_b32 v64, v66\n"
    "v_mov_b32 v52, v60\n"
    "v_mov_b32 v53, v61\n"
    "v_mov_b32 v54, v62\n"
    "v_mov_b32 v55, v63\n"
    "v_mov_b32 v65, v67\n"
    "s_branch .Ltail\n"
    ".Ltail1:\n"
    "v_add_u32 v84, 32768, v73\n"
    "v_cvt_pk_bf16_f32 v54, v28, v29\n"
    "v_cvt_pk_bf16_f32 v55, v30, v31\n"
    "s_branch .Ltail\n"
    ".Ltail2:\n"
    "v_add_u32 v84, 49152, v73\n"
    "v_cvt_pk_bf16_f32 v62, v28, v29\n"
    "v_cvt_pk_bf16_f32 v63, v30, v31\n"
    "s_nop 0\n"
    "v_mov_b32 v48, v56\n"
    "v_mov_b32 v49, v57\n"
    "v_mov_b32 v50, v58\n"
    "v_mov_b32 v51, v59\n"
    "v_mov_b32 v64, v66\n"
    "v_mov_b32 v52, v60\n"
    "v_mov_b32 v53, v61\n"
    "v_mov_b32 v54, v62\n"
    "v_mov_b32 v55, v63\n"
    "v_mov_b32 v65, v67\n"
    "s_branch .Ltail\n"
    ".Ltail3:\n"
    "v_add_u32 v84, 0, v73\n"
    "v_cvt_pk_bf16_f32 v54, v28, v29\n"
    "v_cvt_pk_bf16_f32 v55, v30, v31\n"
    ".Ltail:\n"
    "ds_read_b64_tr_b16 a[128:129], v84 offset:0\n"
    "ds_read_b64_tr_b16 a[130:131], v84 offset:8192\n"
    "ds_read_b64_tr_b16 a[132:133], v84 offset:256\n"
    "ds_read_b64_tr_b16 a[134:135], v84 offset:8448\n"
    "ds_read_b64_tr_b16 a[136:137], v84 offset:512\n"
    "ds_read_b64_tr_b16 a[138:139], v84 offset:8704\n"
    "ds_read_b64_tr_b16 a[140:141], v84 offset:768\n"
    "ds_read_b64_tr_b16 a[142:143], v84 offset:8960\n"
    "ds_read_b64_tr_b16 a[144:145], v84 offset:1024\n"
    "ds_read_b64_tr_b16 a[146:147], v84 offset:9216\n"
    "ds_read_b64_tr_b16 a[148:149], v84 offset:1280\n"
    "ds_read_b64_tr_b16 a[150:151], v84 offset:9472\n"
    "ds_read_b64_tr_b16 a[152:153], v84 offset:1536\n"
    "ds_read_b64_tr_b16 a[154:155], v84 offset:9728\n"
    "ds_read_b64_tr_b16 a[156:157], v84 offset:1792\n"
    "ds_read_b64_tr_b16 a[158:159], v84 offset:9984\n"
    "ds_read_b64_tr_b16 a[160:161], v84 offset:2048\n"
    "ds_read_b64_tr_b16 a[162:163], v84 offset:10240\n"
    "ds_read_b64_tr_b16 a[164:165], v84 offset:2304\n"
    "ds_read_b64_tr_b16 a[166:167], v84 offset:10496\n"
    "ds_read_b64_tr_b16 a[168:169], v84 offset:2560\n"
    "ds_read_b64_tr_b16 a[170:171], v84 offset:10752\n"
    "ds_read_b64_tr_b16 a[172:173], v84 offset:2816\n"
    "ds_read_b64_tr_b16 a[174:175], v84 offset:11008\n"
    "ds_read_b64_tr_b16 a[176:177], v84 offset:3072\n"
    "ds_read_b64_tr_b16 a[178:179], v84 offset:11264\n"
    "ds_read_b64_tr_b16 a[180:181], v84 offset:3328\n"
    "ds_read_b64_tr_b16 a[182:183], v84 offset:11520\n"
    "ds_read_b64_tr_b16 a[184:185], v84 offset:3584\n"
    "ds_read_b64_tr_b16 a[186:187], v84 offset:11776\n"
    "ds_read_b64_tr_b16 a[188:189], v84 offset:3840\n"
    "ds_read_b64_tr_b16 a[190:191], v84 offset:12032\n"
    "v_mov_b32 v112, v64\n"
    "v_mov_b32 v113, v65\n"
    "s_nop 0\n"
    "v_permlane16_swap_b32 v64, v112\n"
    "v_permlane16_swap_b32 v65, v113\n"
    "s_nop 0\n"
    "v_add_f32 v64, v64, v112\n"
    "v_add_f32 v65, v65, v113\n"
    "v_mov_b32 v112, v64\n"
    "v_mov_b32 v113, v65\n"
    "s_nop 0\n"
    "v_permlane32_swap_b32 v64, v112\n"
    "v_permlane32_swap_b32 v65, v113\n"
    "s_nop 0\n"
    "v_add_f32 v64, v64, v112\n"
    "v_add_f32 v65, v65, v113\n"
    "v_rcp_f32 v64, v64\n"
    "v_rcp_f32 v65, v65\n"
    "s_waitcnt vmcnt(0)\n"
    "s_waitcnt lgkmcnt(15)\n"
    "v_mfma_f32_16x16x32_bf16 v[128:131], a[128:131], v[48:51], v[128:131]\n"
    "v_mfma_f32_16x16x32_bf16 v[132:135], a[128:131], v[52:55], v[132:135]\n"
    "v_mfma_f32_16x16x32_bf16 v[136:139], a[132:135], v[48:51], v[136:139]\n"
    "v_mfma_f32_16x16x32_bf16 v[140:143], a[132:135], v[52:55], v[140:143]\n"
    "v_mfma_f32_16x16x32_bf16 v[144:147], a[136:139], v[48:51], v[144:147]\n"
    "v_mfma_f32_16x16x32_bf16 v[148:151], a[136:139], v[52:55], v[148:151]\n"
    "v_mfma_f32_16x16x32_bf16 v[152:155], a[140:143], v[48:51], v[152:155]\n"
    "v_mfma_f32_16x16x32_bf16 v[156:159], a[140:143], v[52:55], v[156:159]\n"
    "s_waitcnt lgkmcnt(0)\n"
    "v_mfma_f32_16x16x32_bf16 v[160:163], a[144:147], v[48:51], v[160:163]\n"
    "v_mul_f32 v128, v128, v64\n"
    "v_mul_f32 v129, v129, v64\n"
    "v_mul_f32 v130, v130, v64\n"
    "v_mul_f32 v131, v131, v64\n"
    "v_cvt_pk_bf16_f32 v104, v128, v129\n"
    "v_cvt_pk_bf16_f32 v105, v130, v131\n"
    "v_mfma_f32_16x16x32_bf16 v[164:167], a[144:147], v[52:55], v[164:167]\n"
    "ds_write_b64 v85, v[104:105] offset:0\n"
    "v_mul_f32 v132, v132, v65\n"
    "v_mul_f32 v133, v133, v65\n"
    "v_mul_f32 v134, v134, v65\n"
    "v_mul_f32 v135, v135, v65\n"
    "v_cvt_pk_bf16_f32 v106, v132, v133\n"
    "v_cvt_pk_bf16_f32 v107, v134, v135\n"
    "v_mfma_f32_16x16x32_bf16 v[168:171], a[148:151], v[48:51], v[168:171]\n"
    "ds_write_b64 v85, v[106:107] offset:32768\n"
    "v_mul_f32 v136, v136, v64\n"
    "v_mul_f32 v137, v137, v64\n"
    "v_mul_f32 v138, v138, v64\n"
    "v_mul_f32 v139, v139, v64\n"
    "v_cvt_pk_bf16_f32 v108, v136, v137\n"
    "v_cvt_pk_bf16_f32 v109, v138, v139\n"
    "v_mfma_f32_16x16x32_bf16 v[172:175], a[148:151], v[52:55], v[172:175]\n"
    "ds_write_b64 v86, v[108:109] offset:0\n"
    "v_mul_f32 v140, v140, v65\n"
    "v_mul_f32 v141, v141, v65\n"
    "v_mul_f32 v142, v142, v65\n"
    "v_mul_f32 v143, v143, v65\n"
    "v_cvt_pk_bf16_f32 v110, v140, v141\n"
    "v_cvt_pk_bf16_f32 v111, v142, v143\n"
    "v_mfma_f32_16x16x32_bf16 v[176:179], a[152:155], v[48:51], v[176:179]\n"
    "ds_write_b64 v86, v[110:111] offset:32768\n"
    "v_mul_f32 v144, v144, v64\n"
    "v_mul_f32 v145, v145, v64\n"
    "v_mul_f32 v146, v146, v64\n"
    "v_mul_f32 v147, v147, v64\n"
    "v_cvt_pk_bf16_f32 v104, v144, v145\n"
    "v_cvt_pk_bf16_f32 v105, v146, v147\n"
    "v_mfma_f32_16x16x32_bf16 v[180:183], a[152:155], v[52:55], v[180:183]\n"
    "ds_write_b64 v87, v[104:105] offset:0\n"
    "v_mul_f32 v148, v148, v65\n"
    "v_mul_f32 v149, v149, v65\n"
    "v_mul_f32 v150, v150, v65\n"
    "v_mul_f32 v151, v151, v65\n"
    "v_cvt_pk_bf16_f32 v106, v148, v149\n"
    "v_cvt_pk_bf16_f32 v107, v150, v151\n"
    "v_mfma_f32_16x16x32_bf16 v[184:187], a[156:159], v[48:51], v[184:187]\n"
    "ds_write_b64 v87, v[106:107] offset:32768\n"
    "v_mul_f32 v152, v152, v64\n"
    "v_mul_f32 v153, v153, v64\n"
    "v_mul_f32 v154, v154, v64\n"
    "v_mul_f32 v155, v155, v64\n"
    "v_cvt_pk_bf16_f32 v108, v152, v153\n"
    "v_cvt_pk_bf16_f32 v109, v154, v155\n"
    "v_mfma_f32_16x16x32_bf16 v[188:191], a[156:159], v[52:55], v[188:191]\n"
    "ds_write_b64 v88, v[108:109] offset:0\n"
    "v_mul_f32 v156, v156, v65\n"
    "v_mul_f32 v157, v157, v65\n"
    "v_mul_f32 v158, v158, v65\n"
    "v_mul_f32 v159, v159, v65\n"
    "v_cvt_pk_bf16_f32 v110, v156, v157\n"
    "v_cvt_pk_bf16_f32 v111, v158, v159\n"
    "v_mfma_f32_16x16x32_bf16 v[192:195], a[160:163], v[48:51], v[192:195]\n"
    "ds_write_b64 v88, v[110:111] offset:32768\n"
    "ds_read_b128 v[112:115], v89 offset:0\n"
    "ds_read_b128 v[116:119], v89 offset:16384\n"
    "ds_read_b128 v[120:123], v89 offset:32768\n"
    "ds_read_b128 v[124:127], v89 offset:49152\n"
    "v_mul_f32 v160, v160, v64\n"
    "v_mul_f32 v161, v161, v64\n"
    "v_mul_f32 v162, v162, v64\n"
    "v_mul_f32 v163, v163, v64\n"
    "v_cvt_pk_bf16_f32 v104, v160, v161\n"
    "v_cvt_pk_bf16_f32 v105, v162, v163\n"
    "v_mfma_f32_16x16x32_bf16 v[196:199], a[160:163], v[52:55], v[196:199]\n"
    "ds_write_b64 v85, v[104:105] offset:128\n"
    "v_mul_f32 v164, v164, v65\n"
    "v_mul_f32 v165, v165, v65\n"
    "v_mul_f32 v166, v166, v65\n"
    "v_mul_f32 v167, v167, v65\n"
    "v_cvt_pk_bf16_f32 v106, v164, v165\n"
    "v_cvt_pk_bf16_f32 v107, v166, v167\n"
    "v_mfma_f32_16x16x32_bf16 v[200:203], a[164:167], v[48:51], v[200:203]\n"
    "ds_write_b64 v85, v[106:107] offset:32896\n"
    "s_waitcnt lgkmcnt(5)\n"
    "global_store_dwordx4 v90, v[112:115], s[92:93] offset:0 sc1\n"
    "s_waitcnt lgkmcnt(4)\n"
    "global_store_dwordx4 v91, v[116:119], s[92:93] offset:0 sc1\n"
    "s_waitcnt lgkmcnt(3)\n"
    "global_store_dwordx4 v92, v[120:123], s[92:93] offset:0 sc1\n"
    "s_waitcnt lgkmcnt(2)\n"
    "global_store_dwordx4 v93, v[124:127], s[92:93] offset:0 sc1\n"
    "v_mul_f32 v168, v168, v64\n"
    "v_mul_f32 v169, v169, v64\n"
    "v_mul_f32 v170, v170, v64\n"
    "v_mul_f32 v171, v171, v64\n"
    "v_cvt_pk_bf16_f32 v108, v168, v169\n"
    "v_cvt_pk_bf16_f32 v109, v170, v171\n"
    "v_mfma_f32_16x16x32_bf16 v[204:207], a[164:167], v[52:55], v[204:207]\n"
    "ds_write_b64 v86, v[108:109] offset:128\n"
    "v_mul_f32 v172, v172, v65\n"
    "v_mul_f32 v173, v173, v65\n"
    "v_mul_f32 v174, v174, v65\n"
    "v_mul_f32 v175, v175, v65\n"
    "v_cvt_pk_bf16_f32 v110, v172, v173\n"
    "v_cvt_pk_bf16_f32 v111, v174, v175\n"
    "v_mfma_f32_16x16x32_bf16 v[208:211], a[168:171], v[48:51], v[208:211]\n"
    "ds_write_b64 v86, v[110:111] offset:32896\n"
    "v_mul_f32 v176, v176, v64\n"
    "v_mul_f32 v177, v177, v64\n"
    "v_mul_f32 v178, v178, v64\n"
    "v_mul_f32 v179, v179, v64\n"
    "v_cvt_pk_bf16_f32 v104, v176, v177\n"
    "v_cvt_pk_bf16_f32 v105, v178, v179\n"
    "v_mfma_f32_16x16x32_bf16 v[212:215], a[168:171], v[52:55], v[212:215]\n"
    "ds_write_b64 v87, v[104:105] offset:128\n"
    "v_mul_f32 v180, v180, v65\n"
    "v_mul_f32 v181, v181, v65\n"
    "v_mul_f32 v182, v182, v65\n"
    "v_mul_f32 v183, v183, v65\n"
    "v_cvt_pk_bf16_f32 v106, v180, v181\n"
    "v_cvt_pk_bf16_f32 v107, v182, v183\n"
    "v_mfma_f32_16x16x32_bf16 v[216:219], a[172:175], v[48:51], v[216:219]\n"
    "ds_write_b64 v87, v[106:107] offset:32896\n"
    "v_mul_f32 v184, v184, v64\n"
    "v_mul_f32 v185, v185, v64\n"
    "v_mul_f32 v186, v186, v64\n"
    "v_mul_f32 v187, v187, v64\n"
    "v_cvt_pk_bf16_f32 v108, v184, v185\n"
    "v_cvt_pk_bf16_f32 v109, v186, v187\n"
    "v_mfma_f32_16x16x32_bf16 v[220:223], a[172:175], v[52:55], v[220:223]\n"
    "ds_write_b64 v88, v[108:109] offset:128\n"
    "v_mul_f32 v188, v188, v65\n"
    "v_mul_f32 v189, v189, v65\n"
    "v_mul_f32 v190, v190, v65\n"
    "v_mul_f32 v191, v191, v65\n"
    "v_cvt_pk_bf16_f32 v110, v188, v189\n"
    "v_cvt_pk_bf16_f32 v111, v190, v191\n"
    "v_mfma_f32_16x16x32_bf16 v[224:227], a[176:179], v[48:51], v[224:227]\n"
    "ds_write_b64 v88, v[110:111] offset:32896\n"
    "ds_read_b128 v[112:115], v89 offset:128\n"
    "ds_read_b128 v[116:119], v89 offset:16512\n"
    "ds_read_b128 v[120:123], v89 offset:32896\n"
    "ds_read_b128 v[124:127], v89 offset:49280\n"
    "v_mul_f32 v192, v192, v64\n"
    "v_mul_f32 v193, v193, v64\n"
    "v_mul_f32 v194, v194, v64\n"
    "v_mul_f32 v195, v195, v64\n"
    "v_cvt_pk_bf16_f32 v104, v192, v193\n"
    "v_cvt_pk_bf16_f32 v105, v194, v195\n"
    "v_mfma_f32_16x16x32_bf16 v[228:231], a[176:179], v[52:55], v[228:231]\n"
    "ds_write_b64 v85, v[104:105] offset:256\n"
    "v_mul_f32 v196, v196, v65\n"
    "v_mul_f32 v197, v197, v65\n"
    "v_mul_f32 v198, v198, v65\n"
    "v_mul_f32 v199, v199, v65\n"
    "v_cvt_pk_bf16_f32 v106, v196, v197\n"
    "v_cvt_pk_bf16_f32 v107, v198, v199\n"
    "v_mfma_f32_16x16x32_bf16 v[232:235], a[180:183], v[48:51], v[232:235]\n"
    "ds_write_b64 v85, v[106:107] offset:33024\n"
    "s_waitcnt lgkmcnt(5)\n"
    "global_store_dwordx4 v90, v[112:115], s[92:93] offset:128 sc1\n"
    "s_waitcnt lgkmcnt(4)\n"
    "global_store_dwordx4 v91, v[116:119], s[92:93] offset:128 sc1\n"
    "s_waitcnt lgkmcnt(3)\n"
    "global_store_dwordx4 v92, v[120:123], s[92:93] offset:128 sc1\n"
    "s_waitcnt lgkmcnt(2)\n"
    "global_store_dwordx4 v93, v[124:127], s[92:93] offset:128 sc1\n"
    "v_mul_f32 v200, v200, v64\n"
    "v_mul_f32 v201, v201, v64\n"
    "v_mul_f32 v202, v202, v64\n"
    "v_mul_f32 v203, v203, v64\n"
    "v_cvt_pk_bf16_f32 v108, v200, v201\n"
    "v_cvt_pk_bf16_f32 v109, v202, v203\n"
    "v_mfma_f32_16x16x32_bf16 v[236:239], a[180:183], v[52:55], v[236:239]\n"
    "ds_write_b64 v86, v[108:109] offset:256\n"
    "v_mul_f32 v204, v204, v65\n"
    "v_mul_f32 v205, v205, v65\n"
    "v_mul_f32 v206, v206, v65\n"
    "v_mul_f32 v207, v207, v65\n"
    "v_cvt_pk_bf16_f32 v110, v204, v205\n"
    "v_cvt_pk_bf16_f32 v111, v206, v207\n"
    "v_mfma_f32_16x16x32_bf16 v[240:243], a[184:187], v[48:51], v[240:243]\n"
    "ds_write_b64 v86, v[110:111] offset:33024\n"
    "v_mul_f32 v208, v208, v64\n"
    "v_mul_f32 v209, v209, v64\n"
    "v_mul_f32 v210, v210, v64\n"
    "v_mul_f32 v211, v211, v64\n"
    "v_cvt_pk_bf16_f32 v104, v208, v209\n"
    "v_cvt_pk_bf16_f32 v105, v210, v211\n"
    "v_mfma_f32_16x16x32_bf16 v[244:247], a[184:187], v[52:55], v[244:247]\n"
    "ds_write_b64 v87, v[104:105] offset:256\n"
    "v_mul_f32 v212, v212, v65\n"
    "v_mul_f32 v213, v213, v65\n"
    "v_mul_f32 v214, v214, v65\n"
    "v_mul_f32 v215, v215, v65\n"
    "v_cvt_pk_bf16_f32 v106, v212, v213\n"
    "v_cvt_pk_bf16_f32 v107, v214, v215\n"
    "v_mfma_f32_16x16x32_bf16 v[248:251], a[188:191], v[48:51], v[248:251]\n"
    "ds_write_b64 v87, v[106:107] offset:33024\n"
    "v_mul_f32 v216, v216, v64\n"
    "v_mul_f32 v217, v217, v64\n"
    "v_mul_f32 v218, v218, v64\n"
    "v_mul_f32 v219, v219, v64\n"
    "v_cvt_pk_bf16_f32 v108, v216, v217\n"
    "v_cvt_pk_bf16_f32 v109, v218, v219\n"
    "v_mfma_f32_16x16x32_bf16 v[252:255], a[188:191], v[52:55], v[252:255]\n"
    "ds_write_b64 v88, v[108:109] offset:256\n"
    "v_mul_f32 v220, v220, v65\n"
    "v_mul_f32 v221, v221, v65\n"
    "v_mul_f32 v222, v222, v65\n"
    "v_mul_f32 v223, v223, v65\n"
    "v_cvt_pk_bf16_f32 v110, v220, v221\n"
    "v_cvt_pk_bf16_f32 v111, v222, v223\n"
    "ds_write_b64 v88, v[110:111] offset:33024\n"
    "ds_read_b128 v[112:115], v89 offset:256\n"
    "ds_read_b128 v[116:119], v89 offset:16640\n"
    "ds_read_b128 v[120:123], v89 offset:33024\n"
    "ds_read_b128 v[124:127], v89 offset:49408\n"
    "v_mul_f32 v224, v224, v64\n"
    "v_mul_f32 v225, v225, v64\n"
    "v_mul_f32 v226, v226, v64\n"
    "v_mul_f32 v227, v227, v64\n"
    "v_cvt_pk_bf16_f32 v104, v224, v225\n"
    "v_cvt_pk_bf16_f32 v105, v226, v227\n"
    "ds_write_b64 v85, v[104:105] offset:384\n"
    "v_mul_f32 v228, v228, v65\n"
    "v_mul_f32 v229, v229, v65\n"
    "v_mul_f32 v230, v230, v65\n"
    "v_mul_f32 v231, v231, v65\n"
    "v_cvt_pk_bf16_f32 v106, v228, v229\n"
    "v_cvt_pk_bf16_f32 v107, v230, v231\n"
    "ds_write_b64 v85, v[106:107] offset:33152\n"
    "s_waitcnt lgkmcnt(5)\n"
    "global_store_dwordx4 v90, v[112:115], s[92:93] offset:256 sc1\n"
    "s_waitcnt lgkmcnt(4)\n"
    "global_store_dwordx4 v91, v[116:119], s[92:93] offset:256 sc1\n"
    "s_waitcnt lgkmcnt(3)\n"
    "global_store_dwordx4 v92, v[120:123], s[92:93] offset:256 sc1\n"
    "s_waitcnt lgkmcnt(2)\n"
    "global_store_dwordx4 v93, v[124:127], s[92:93] offset:256 sc1\n"
    "v_mul_f32 v232, v232, v64\n"
    "v_mul_f32 v233, v233, v64\n"
    "v_mul_f32 v234, v234, v64\n"
    "v_mul_f32 v235, v235, v64\n"
    "v_cvt_pk_bf16_f32 v108, v232, v233\n"
    "v_cvt_pk_bf16_f32 v109, v234, v235\n"
    "ds_write_b64 v86, v[108:109] offset:384\n"
    "v_mul_f32 v236, v236, v65\n"
    "v_mul_f32 v237, v237, v65\n"
    "v_mul_f32 v238, v238, v65\n"
    "v_mul_f32 v239, v239, v65\n"
    "v_cvt_pk_bf16_f32 v110, v236, v237\n"
    "v_cvt_pk_bf16_f32 v111, v238, v239\n"
    "ds_write_b64 v86, v[110:111] offset:33152\n"
    "v_mul_f32 v240, v240, v64\n"
    "v_mul_f32 v241, v241, v64\n"
    "v_mul_f32 v242, v242, v64\n"
    "v_mul_f32 v243, v243, v64\n"
    "v_cvt_pk_bf16_f32 v104, v240, v241\n"
    "v_cvt_pk_bf16_f32 v105, v242, v243\n"
    "ds_write_b64 v87, v[104:105] offset:384\n"
    "v_mul_f32 v244, v244, v65\n"
    "v_mul_f32 v245, v245, v65\n"
    "v_mul_f32 v246, v246, v65\n"
    "v_mul_f32 v247, v247, v65\n"
    "v_cvt_pk_bf16_f32 v106, v244, v245\n"
    "v_cvt_pk_bf16_f32 v107, v246, v247\n"
    "ds_write_b64 v87, v[106:107] offset:33152\n"
    "v_mul_f32 v248, v248, v64\n"
    "v_mul_f32 v249, v249, v64\n"
    "v_mul_f32 v250, v250, v64\n"
    "v_mul_f32 v251, v251, v64\n"
    "v_cvt_pk_bf16_f32 v108, v248, v249\n"
    "v_cvt_pk_bf16_f32 v109, v250, v251\n"
    "ds_write_b64 v88, v[108:109] offset:384\n"
    "v_mul_f32 v252, v252, v65\n"
    "v_mul_f32 v253, v253, v65\n"
    "v_mul_f32 v254, v254, v65\n"
    "v_mul_f32 v255, v255, v65\n"
    "v_cvt_pk_bf16_f32 v110, v252, v253\n"
    "v_cvt_pk_bf16_f32 v111, v254, v255\n"
    "ds_write_b64 v88, v[110:111] offset:33152\n"
    "ds_read_b128 v[112:115], v89 offset:384\n"
    "ds_read_b128 v[116:119], v89 offset:16768\n"
    "ds_read_b128 v[120:123], v89 offset:33152\n"
    "ds_read_b128 v[124:127], v89 offset:49536\n"
    "s_waitcnt lgkmcnt(3)\n"
    "global_store_dwordx4 v90, v[112:115], s[92:93] offset:384 sc1\n"
    "s_waitcnt lgkmcnt(2)\n"
    "global_store_dwordx4 v91, v[116:119], s[92:93] offset:384 sc1\n"
    "s_waitcnt lgkmcnt(1)\n"
    "global_store_dwordx4 v92, v[120:123], s[92:93] offset:384 sc1\n"
    "s_waitcnt lgkmcnt(0)\n"
    "global_store_dwordx4 v93, v[124:127], s[92:93] offset:384 sc1\n"
    ".Lend:\n"
    "s_endpgm\n"
    ".Lmask0:\n"
    "s_swappc_b64 s[56:57], s[54:55]\n"
    "s_branch .Lnomask0\n"
    ".Lslow0:\n"
    "s_nop 7\n"
    "s_nop 7\n"
    "v_max3_f32 v112, v0, v1, v2\n"
    "v_max3_f32 v112, v112, v3, v8\n"
    "v_max3_f32 v112, v112, v9, v10\n"
    "v_max_f32 v112, v112, v11\n"
    "v_log_f32 v113, v66\n"
    "s_nop 0\n"
    "v_max3_f32 v112, v112, v113, 0\n"
    "v_mov_b32 v113, v112\n"
    "s_nop 1\n"
    "v_permlane16_swap_b32 v112, v113\n"
    "s_nop 1\n"
    "v_max_f32 v112, v112, v113\n"
    "v_mov_b32 v113, v112\n"
    "s_nop 1\n"
    "v_permlane32_swap_b32 v112, v113\n"
    "s_nop 1\n"
    "v_max_f32 v112, v112, v113\n"
    "v_exp_f32 v114, -v112\n"
    "s_nop 0\n"
    "v_mul_f32 v128, v128, v114\n"
    "v_mul_f32 v129, v129, v114\n"
    "v_mul_f32 v130, v130, v114\n"
    "v_mul_f32 v131, v131, v114\n"
    "v_mul_f32 v136, v136, v114\n"
    "v_mul_f32 v137, v137, v114\n"
    "v_mul_f32 v138, v138, v114\n"
    "v_mul_f32 v139, v139, v114\n"
    "v_mul_f32 v144, v144, v114\n"
    "v_mul_f32 v145, v145, v114\n"
    "v_mul_f32 v146, v146, v114\n"
    "v_mul_f32 v147, v147, v114\n"
    "v_mul_f32 v152, v152, v114\n"
    "v_mul_f32 v153, v153, v114\n"
    "v_mul_f32 v154, v154, v114\n"
    "v_mul_f32 v155, v155, v114\n"
    "v_mul_f32 v160, v160, v114\n"
    "v_mul_f32 v161, v161, v114\n"
    "v_mul_f32 v162, v162, v114\n"
    "v_mul_f32 v163, v163, v114\n"
    "v_mul_f32 v168, v168, v114\n"
    "v_mul_f32 v169, v169, v114\n"
    "v_mul_f32 v170, v170, v114\n"
    "v_mul_f32 v171, v171, v114\n"
    "v_mul_f32 v176, v176, v114\n"
    "v_mul_f32 v177, v177, v114\n"
    "v_mul_f32 v178, v178, v114\n"
    "v_mul_f32 v179, v179, v114\n"
    "v_mul_f32 v184, v184, v114\n"
    "v_mul_f32 v185, v185, v114\n"
    "v_mul_f32 v186, v186, v114\n"
    "v_mul_f32 v187, v187, v114\n"
    "v_mul_f32 v192, v192, v114\n"
    "v_mul_f32 v193, v193, v114\n"
    "v_mul_f32 v194, v194, v114\n"
    "v_mul_f32 v195, v195, v114\n"
    "v_mul_f32 v200, v200, v114\n"
    "v_mul_f32 v201, v201, v114\n"
    "v_mul_f32 v202, v202, v114\n"
    "v_mul_f32 v203, v203, v114\n"
    "v_mul_f32 v208, v208, v114\n"
    "v_mul_f32 v209, v209, v114\n"
    "v_mul_f32 v210, v210, v114\n"
    "v_mul_f32 v211, v211, v114\n"
    "v_mul_f32 v216, v216, v114\n"
    "v_mul_f32 v217, v217, v114\n"
    "v_mul_f32 v218, v218, v114\n"
    "v_mul_f32 v219, v219, v114\n"
    "v_mul_f32 v224, v224, v114\n"
    "v_mul_f32 v225, v225, v114\n"
    "v_mul_f32 v226, v226, v114\n"
    "v_mul_f32 v227, v227, v114\n"
    "v_mul_f32 v232, v232, v114\n"
    "v_mul_f32 v233, v233, v114\n"
    "v_mul_f32 v234, v234, v114\n"
    "v_mul_f32 v235, v235, v114\n"
    "v_mul_f32 v240, v240, v114\n"
    "v_mul_f32 v241, v241, v114\n"
    "v_mul_f32 v242, v242, v114\n"
    "v_mul_f32 v243, v243, v114\n"
    "v_mul_f32 v248, v248, v114\n"
    "v_mul_f32 v249, v249, v114\n"
    "v_mul_f32 v250, v250, v114\n"
    "v_mul_f32 v251, v251, v114\n"
    "v_mul_f32 v64, v66, v114\n"
    "v_sub_f32 v16, v0, v112\n"
    "v_sub_f32 v17, v1, v112\n"
    "v_sub_f32 v18, v2, v112\n"
    "v_sub_f32 v19, v3, v112\n"
    "v_sub_f32 v24, v8, v112\n"
    "v_sub_f32 v25, v9, v112\n"
    "v_sub_f32 v26, v10, v112\n"
    "v_sub_f32 v27, v11, v112\n"
    "v_exp_f32 v16, v16\n"
    "v_exp_f32 v17, v17\n"
    "v_exp_f32 v18, v18\n"
    "v_exp_f32 v19, v19\n"
    "v_exp_f32 v24, v24\n"
    "v_exp_f32 v25, v25\n"
    "v_exp_f32 v26, v26\n"
    "v_exp_f32 v27, v27\n"
    "v_sub_f32 v32, v32, v112\n"
    "v_sub_f32 v33, v33, v112\n"
    "v_sub_f32 v34, v34, v112\n"
    "v_sub_f32 v35, v35, v112\n"
    "v_sub_f32 v40, v40, v112\n"
    "v_sub_f32 v41, v41, v112\n"
    "v_sub_f32 v42, v42, v112\n"
    "v_sub_f32 v43, v43, v112\n"
    "v_add_f32 v64, v64, v16\n"
    "v_add_f32 v64, v64, v17\n"
    "v_add_f32 v64, v64, v18\n"
    "v_add_f32 v64, v64, v19\n"
    "v_add_f32 v64, v64, v24\n"
    "v_add_f32 v64, v64, v25\n"
    "v_add_f32 v64, v64, v26\n"
    "v_add_f32 v64, v64, v27\n"
    "v_cvt_pk_bf16_f32 v48, v16, v17\n"
    "v_cvt_pk_bf16_f32 v49, v18, v19\n"
    "v_cvt_pk_bf16_f32 v50, v24, v25\n"
    "v_cvt_pk_bf16_f32 v51, v26, v27\n"
    "v_max3_f32 v112, v4, v5, v6\n"
    "v_max3_f32 v112, v112, v7, v12\n"
    "v_max3_f32 v112, v112, v13, v14\n"
    "v_max_f32 v112, v112, v15\n"
    "v_log_f32 v113, v67\n"
    "s_nop 0\n"
    "v_max3_f32 v112, v112, v113, 0\n"
    "v_mov_b32 v113, v112\n"
    "s_nop 1\n"
    "v_permlane16_swap_b32 v112, v113\n"
    "s_nop 1\n"
    "v_max_f32 v112, v112, v113\n"
    "v_mov_b32 v113, v112\n"
    "s_nop 1\n"
    "v_permlane32_swap_b32 v112, v113\n"
    "s_nop 1\n"
    "v_max_f32 v112, v112, v113\n"
    "v_exp_f32 v114, -v112\n"
    "s_nop 0\n"
    "v_mul_f32 v132, v132, v114\n"
    "v_mul_f32 v133, v133, v114\n"
    "v_mul_f32 v134, v134, v114\n"
    "v_mul_f32 v135, v135, v114\n"
    "v_mul_f32 v140, v140, v114\n"
    "v_mul_f32 v141, v141, v114\n"
    "v_mul_f32 v142, v142, v114\n"
    "v_mul_f32 v143, v143, v114\n"
    "v_mul_f32 v148, v148, v114\n"
    "v_mul_f32 v149, v149, v114\n"
    "v_mul_f32 v150, v150, v114\n"
    "v_mul_f32 v151, v151, v114\n"
    "v_mul_f32 v156, v156, v114\n"
    "v_mul_f32 v157, v157, v114\n"
    "v_mul_f32 v158, v158, v114\n"
    "v_mul_f32 v159, v159, v114\n"
    "v_mul_f32 v164, v164, v114\n"
    "v_mul_f32 v165, v165, v114\n"
    "v_mul_f32 v166, v166, v114\n"
    "v_mul_f32 v167, v167, v114\n"
    "v_mul_f32 v172, v172, v114\n"
    "v_mul_f32 v173, v173, v114\n"
    "v_mul_f32 v174, v174, v114\n"
    "v_mul_f32 v175, v175, v114\n"
    "v_mul_f32 v180, v180, v114\n"
    "v_mul_f32 v181, v181, v114\n"
    "v_mul_f32 v182, v182, v114\n"
    "v_mul_f32 v183, v183, v114\n"
    "v_mul_f32 v188, v188, v114\n"
    "v_mul_f32 v189, v189, v114\n"
    "v_mul_f32 v190, v190, v114\n"
    "v_mul_f32 v191, v191, v114\n"
    "v_mul_f32 v196, v196, v114\n"
    "v_mul_f32 v197, v197, v114\n"
    "v_mul_f32 v198, v198, v114\n"
    "v_mul_f32 v199, v199, v114\n"
    "v_mul_f32 v204, v204, v114\n"
    "v_mul_f32 v205, v205, v114\n"
    "v_mul_f32 v206, v206, v114\n"
    "v_mul_f32 v207, v207, v114\n"
    "v_mul_f32 v212, v212, v114\n"
    "v_mul_f32 v213, v213, v114\n"
    "v_mul_f32 v214, v214, v114\n"
    "v_mul_f32 v215, v215, v114\n"
    "v_mul_f32 v220, v220, v114\n"
    "v_mul_f32 v221, v221, v114\n"
    "v_mul_f32 v222, v222, v114\n"
    "v_mul_f32 v223, v223, v114\n"
    "v_mul_f32 v228, v228, v114\n"
    "v_mul_f32 v229, v229, v114\n"
    "v_mul_f32 v230, v230, v114\n"
    "v_mul_f32 v231, v231, v114\n"
    "v_mul_f32 v236, v236, v114\n"
    "v_mul_f32 v237, v237, v114\n"
    "v_mul_f32 v238, v238, v114\n"
    "v_mul_f32 v239, v239, v114\n"
    "v_mul_f32 v244, v244, v114\n"
    "v_mul_f32 v245, v245, v114\n"
    "v_mul_f32 v246, v246, v114\n"
    "v_mul_f32 v247, v247, v114\n"
    "v_mul_f32 v252, v252, v114\n"
    "v_mul_f32 v253, v253, v114\n"
    "v_mul_f32 v254, v254, v114\n"
    "v_mul_f32 v255, v255, v114\n"
    "v_mul_f32 v65, v67, v114\n"
    "v_sub_f32 v20, v4, v112\n"
    "v_sub_f32 v21, v5, v112\n"
    "v_sub_f32 v22, v6, v112\n"
    "v_sub_f32 v23, v7, v112\n"
    "v_sub_f32 v28, v12, v112\n"
    "v_sub_f32 v29, v13, v112\n"
    "v_sub_f32 v30, v14, v112\n"
    "v_sub_f32 v31, v15, v112\n"
    "v_exp_f32 v20, v20\n"
    "v_exp_f32 v21, v21\n"
    "v_exp_f32 v22, v22\n"
    "v_exp_f32 v23, v23\n"
    "v_exp_f32 v28, v28\n"
    "v_exp_f32 v29, v29\n"
    "v_exp_f32 v30, v30\n"
    "v_exp_f32 v31, v31\n"
    "v_sub_f32 v36, v36, v112\n"
    "v_sub_f32 v37, v37, v112\n"
    "v_sub_f32 v38, v38, v112\n"
    "v_sub_f32 v39, v39, v112\n"
    "v_sub_f32 v44, v44, v112\n"
    "v_sub_f32 v45, v45, v112\n"
    "v_sub_f32 v46, v46, v112\n"
    "v_sub_f32 v47, v47, v112\n"
    "v_add_f32 v65, v65, v20\n"
    "v_add_f32 v65, v65, v21\n"
    "v_add_f32 v65, v65, v22\n"
    "v_add_f32 v65, v65, v23\n"
    "v_add_f32 v65, v65, v28\n"
    "v_add_f32 v65, v65, v29\n"
    "v_add_f32 v65, v65, v30\n"
    "v_add_f32 v65, v65, v31\n"
    "v_cvt_pk_bf16_f32 v52, v20, v21\n"
    "v_cvt_pk_bf16_f32 v53, v22, v23\n"
    "v_cvt_pk_bf16_f32 v54, v28, v29\n"
    "v_cvt_pk_bf16_f32 v55, v30, v31\n"
    "s_nop 3\n"
    "s_setpc_b64 s[56:57]\n"
    ".Lslow1:\n"
    "s_nop 7\n"
    "s_nop 7\n"
    "v_max3_f32 v112, v0, v1, v2\n"
    "v_max3_f32 v112, v112, v3, v8\n"
    "v_max3_f32 v112, v112, v9, v10\n"
    "v_max_f32 v112, v112, v11\n"
    "v_log_f32 v113, v64\n"
    "s_nop 0\n"
    "v_max3_f32 v112, v112, v113, 0\n"
    "v_mov_b32 v113, v112\n"
    "s_nop 1\n"
    "v_permlane16_swap_b32 v112, v113\n"
    "s_nop 1\n"
    "v_max_f32 v112, v112, v113\n"
    "v_mov_b32 v113, v112\n"
    "s_nop 1\n"
    "v_permlane32_swap_b32 v112, v113\n"
    "s_nop 1\n"
    "v_max_f32 v112, v112, v113\n"
    "v_exp_f32 v114, -v112\n"
    "s_nop 0\n"
    "v_mul_f32 v128, v128, v114\n"
    "v_mul_f32 v129, v129, v114\n"
    "v_mul_f32 v130, v130, v114\n"
    "v_mul_f32 v131, v131, v114\n"
    "v_mul_f32 v136, v136, v114\n"
    "v_mul_f32 v137, v137, v114\n"
    "v_mul_f32 v138, v138, v114\n"
    "v_mul_f32 v139, v139, v114\n"
    "v_mul_f32 v144, v144, v114\n"
    "v_mul_f32 v145, v145, v114\n"
    "v_mul_f32 v146, v146, v114\n"
    "v_mul_f32 v147, v147, v114\n"
    "v_mul_f32 v152, v152, v114\n"
    "v_mul_f32 v153, v153, v114\n"
    "v_mul_f32 v154, v154, v114\n"
    "v_mul_f32 v155, v155, v114\n"
    "v_mul_f32 v160, v160, v114\n"
    "v_mul_f32 v161, v161, v114\n"
    "v_mul_f32 v162, v162, v114\n"
    "v_mul_f32 v163, v163, v114\n"
    "v_mul_f32 v168, v168, v114\n"
    "v_mul_f32 v169, v169, v114\n"
    "v_mul_f32 v170, v170, v114\n"
    "v_mul_f32 v171, v171, v114\n"
    "v_mul_f32 v176, v176, v114\n"
    "v_mul_f32 v177, v177, v114\n"
    "v_mul_f32 v178, v178, v114\n"
    "v_mul_f32 v179, v179, v114\n"
    "v_mul_f32 v184, v184, v114\n"
    "v_mul_f32 v185, v185, v114\n"
    "v_mul_f32 v186, v186, v114\n"
    "v_mul_f32 v187, v187, v114\n"
    "v_mul_f32 v192, v192, v114\n"
    "v_mul_f32 v193, v193, v114\n"
    "v_mul_f32 v194, v194, v114\n"
    "v_mul_f32 v195, v195, v114\n"
    "v_mul_f32 v200, v200, v114\n"
    "v_mul_f32 v201, v201, v114\n"
    "v_mul_f32 v202, v202, v114\n"
    "v_mul_f32 v203, v203, v114\n"
    "v_mul_f32 v208, v208, v114\n"
    "v_mul_f32 v209, v209, v114\n"
    "v_mul_f32 v210, v210, v114\n"
    "v_mul_f32 v211, v211, v114\n"
    "v_mul_f32 v216, v216, v114\n"
    "v_mul_f32 v217, v217, v114\n"
    "v_mul_f32 v218, v218, v114\n"
    "v_mul_f32 v219, v219, v114\n"
    "v_mul_f32 v224, v224, v114\n"
    "v_mul_f32 v225, v225, v114\n"
    "v_mul_f32 v226, v226, v114\n"
    "v_mul_f32 v227, v227, v114\n"
    "v_mul_f32 v232, v232, v114\n"
    "v_mul_f32 v233, v233, v114\n"
    "v_mul_f32 v234, v234, v114\n"
    "v_mul_f32 v235, v235, v114\n"
    "v_mul_f32 v240, v240, v114\n"
    "v_mul_f32 v241, v241, v114\n"
    "v_mul_f32 v242, v242, v114\n"
    "v_mul_f32 v243, v243, v114\n"
    "v_mul_f32 v248, v248, v114\n"
    "v_mul_f32 v249, v249, v114\n"
    "v_mul_f32 v250, v250, v114\n"
    "v_mul_f32 v251, v251, v114\n"
    "v_mul_f32 v66, v64, v114\n"
    "v_sub_f32 v16, v0, v112\n"
    "v_sub_f32 v17, v1, v112\n"
    "v_sub_f32 v18, v2, v112\n"
    "v_sub_f32 v19, v3, v112\n"
    "v_sub_f32 v24, v8, v112\n"
    "v_sub_f32 v25, v9, v112\n"
    "v_sub_f32 v26, v10, v112\n"
    "v_sub_f32 v27, v11, v112\n"
    "v_exp_f32 v16, v16\n"
    "v_exp_f32 v17, v17\n"
    "v_exp_f32 v18, v18\n"
    "v_exp_f32 v19, v19\n"
    "v_exp_f32 v24, v24\n"
    "v_exp_f32 v25, v25\n"
    "v_exp_f32 v26, v26\n"
    "v_exp_f32 v27, v27\n"
    "v_sub_f32 v32, v32, v112\n"
    "v_sub_f32 v33, v33, v112\n"
    "v_sub_f32 v34, v34, v112\n"
    "v_sub_f32 v35, v35, v112\n"
    "v_sub_f32 v40, v40, v112\n"
    "v_sub_f32 v41, v41, v112\n"
    "v_sub_f32 v42, v42, v112\n"
    "v_sub_f32 v43, v43, v112\n"
    "v_add_f32 v66, v66, v16\n"
    "v_add_f32 v66, v66, v17\n"
    "v_add_f32 v66, v66, v18\n"
    "v_add_f32 v66, v66, v19\n"
    "v_add_f32 v66, v66, v24\n"
    "v_add_f32 v66, v66, v25\n"
    "v_add_f32 v66, v66, v26\n"
    "v_add_f32 v66, v66, v27\n"
    "v_cvt_pk_bf16_f32 v56, v16, v17\n"
    "v_cvt_pk_bf16_f32 v57, v18, v19\n"
    "v_cvt_pk_bf16_f32 v58, v24, v25\n"
    "v_cvt_pk_bf16_f32 v59, v26, v27\n"
    "v_max3_f32 v112, v4, v5, v6\n"
    "v_max3_f32 v112, v112, v7, v12\n"
    "v_max3_f32 v112, v112, v13, v14\n"
    "v_max_f32 v112, v112, v15\n"
    "v_log_f32 v113, v65\n"
    "s_nop 0\n"
    "v_max3_f32 v112, v112, v113, 0\n"
    "v_mov_b32 v113, v112\n"
    "s_nop 1\n"
    "v_permlane16_swap_b32 v112, v113\n"
    "s_nop 1\n"
    "v_max_f32 v112, v112, v113\n"
    "v_mov_b32 v113, v112\n"
    "s_nop 1\n"
    "v_permlane32_swap_b32 v112, v113\n"
    "s_nop 1\n"
    "v_max_f32 v112, v112, v113\n"
    "v_exp_f32 v114, -v112\n"
    "s_nop 0\n"
    "v_mul_f32 v132, v132, v114\n"
    "v_mul_f32 v133, v133, v114\n"
    "v_mul_f32 v134, v134, v114\n"
    "v_mul_f32 v135, v135, v114\n"
    "v_mul_f32 v140, v140, v114\n"
    "v_mul_f32 v141, v141, v114\n"
    "v_mul_f32 v142, v142, v114\n"
    "v_mul_f32 v143, v143, v114\n"
    "v_mul_f32 v148, v148, v114\n"
    "v_mul_f32 v149, v149, v114\n"
    "v_mul_f32 v150, v150, v114\n"
    "v_mul_f32 v151, v151, v114\n"
    "v_mul_f32 v156, v156, v114\n"
    "v_mul_f32 v157, v157, v114\n"
    "v_mul_f32 v158, v158, v114\n"
    "v_mul_f32 v159, v159, v114\n"
    "v_mul_f32 v164, v164, v114\n"
    "v_mul_f32 v165, v165, v114\n"
    "v_mul_f32 v166, v166, v114\n"
    "v_mul_f32 v167, v167, v114\n"
    "v_mul_f32 v172, v172, v114\n"
    "v_mul_f32 v173, v173, v114\n"
    "v_mul_f32 v174, v174, v114\n"
    "v_mul_f32 v175, v175, v114\n"
    "v_mul_f32 v180, v180, v114\n"
    "v_mul_f32 v181, v181, v114\n"
    "v_mul_f32 v182, v182, v114\n"
    "v_mul_f32 v183, v183, v114\n"
    "v_mul_f32 v188, v188, v114\n"
    "v_mul_f32 v189, v189, v114\n"
    "v_mul_f32 v190, v190, v114\n"
    "v_mul_f32 v191, v191, v114\n"
    "v_mul_f32 v196, v196, v114\n"
    "v_mul_f32 v197, v197, v114\n"
    "v_mul_f32 v198, v198, v114\n"
    "v_mul_f32 v199, v199, v114\n"
    "v_mul_f32 v204, v204, v114\n"
    "v_mul_f32 v205, v205, v114\n"
    "v_mul_f32 v206, v206, v114\n"
    "v_mul_f32 v207, v207, v114\n"
    "v_mul_f32 v212, v212, v114\n"
    "v_mul_f32 v213, v213, v114\n"
    "v_mul_f32 v214, v214, v114\n"
    "v_mul_f32 v215, v215, v114\n"
    "v_mul_f32 v220, v220, v114\n"
    "v_mul_f32 v221, v221, v114\n"
    "v_mul_f32 v222, v222, v114\n"
    "v_mul_f32 v223, v223, v114\n"
    "v_mul_f32 v228, v228, v114\n"
    "v_mul_f32 v229, v229, v114\n"
    "v_mul_f32 v230, v230, v114\n"
    "v_mul_f32 v231, v231, v114\n"
    "v_mul_f32 v236, v236, v114\n"
    "v_mul_f32 v237, v237, v114\n"
    "v_mul_f32 v238, v238, v114\n"
    "v_mul_f32 v239, v239, v114\n"
    "v_mul_f32 v244, v244, v114\n"
    "v_mul_f32 v245, v245, v114\n"
    "v_mul_f32 v246, v246, v114\n"
    "v_mul_f32 v247, v247, v114\n"
    "v_mul_f32 v252, v252, v114\n"
    "v_mul_f32 v253, v253, v114\n"
    "v_mul_f32 v254, v254, v114\n"
    "v_mul_f32 v255, v255, v114\n"
    "v_mul_f32 v67, v65, v114\n"
    "v_sub_f32 v20, v4, v112\n"
    "v_sub_f32 v21, v5, v112\n"
    "v_sub_f32 v22, v6, v112\n"
    "v_sub_f32 v23, v7, v112\n"
    "v_sub_f32 v28, v12, v112\n"
    "v_sub_f32 v29, v13, v112\n"
    "v_sub_f32 v30, v14, v112\n"
    "v_sub_f32 v31, v15, v112\n"
    "v_exp_f32 v20, v20\n"
    "v_exp_f32 v21, v21\n"
    "v_exp_f32 v22, v22\n"
    "v_exp_f32 v23, v23\n"
    "v_exp_f32 v28, v28\n"
    "v_exp_f32 v29, v29\n"
    "v_exp_f32 v30, v30\n"
    "v_exp_f32 v31, v31\n"
    "v_sub_f32 v36, v36, v112\n"
    "v_sub_f32 v37, v37, v112\n"
    "v_sub_f32 v38, v38, v112\n"
    "v_sub_f32 v39, v39, v112\n"
    "v_sub_f32 v44, v44, v112\n"
    "v_sub_f32 v45, v45, v112\n"
    "v_sub_f32 v46, v46, v112\n"
    "v_sub_f32 v47, v47, v112\n"
    "v_add_f32 v67, v67, v20\n"
    "v_add_f32 v67, v67, v21\n"
    "v_add_f32 v67, v67, v22\n"
    "v_add_f32 v67, v67, v23\n"
    "v_add_f32 v67, v67, v28\n"
    "v_add_f32 v67, v67, v29\n"
    "v_add_f32 v67, v67, v30\n"
    "v_add_f32 v67, v67, v31\n"
    "v_cvt_pk_bf16_f32 v60, v20, v21\n"
    "v_cvt_pk_bf16_f32 v61, v22, v23\n"
    "v_cvt_pk_bf16_f32 v62, v28, v29\n"
    "v_cvt_pk_bf16_f32 v63, v30, v31\n"
    "s_nop 3\n"
    "s_setpc_b64 s[56:57]\n"
    :
    : "s"(__builtin_amdgcn_kernarg_segment_ptr()), "s"(wid), "s"(blockIdx.x), "s"((unsigned)(size_t)lds)
    : "v0", "v1", "v2", "v3", "v4", "v5", "v6", "v7", "v8", "v9", "v10", "v11", "v12", "v13", "v14", "v15", "v16", "v17", "v18", "v19", "v20", "v21", "v22", "v23", "v24", "v25", "v26", "v27", "v28", "v29", "v30", "v31", "v32", "v33", "v34", "v35", "v36", "v37", "v38", "v39", "v40", "v41", "v42", "v43", "v44", "v45", "v46", "v47", "v48", "v49", "v50", "v51", "v52", "v53", "v54", "v55", "v56", "v57", "v58", "v59", "v60", "v61", "v62", "v63", "v64", "v65", "v66", "v67", "v68", "v69", "v70", "v71", "v72", "v73", "v74", "v75", "v76", "v77", "v78", "v79", "v80", "v81", "v82", "v83", "v84", "v85", "v86", "v87", "v88", "v89", "v90", "v91", "v92", "v93", "v94", "v95", "v96", "v97", "v98", "v99", "v100", "v101", "v102", "v103", "v104", "v105", "v106", "v107", "v108", "v109", "v110", "v111", "v112", "v113", "v114", "v115", "v116", "v117", "v118", "v119", "v120", "v121", "v122", "v123", "v124", "v125", "v126", "v127", "v128", "v129", "v130", "v131", "v132", "v133", "v134", "v135", "v136", "v137", "v138", "v139", "v140", "v141", "v142", "v143", "v144", "v145", "v146", "v147", "v148", "v149", "v150", "v151", "v152", "v153", "v154", "v155", "v156", "v157", "v158", "v159", "v160", "v161", "v162", "v163", "v164", "v165", "v166", "v167", "v168", "v169", "v170", "v171", "v172", "v173", "v174", "v175", "v176", "v177", "v178", "v179", "v180", "v181", "v182", "v183", "v184", "v185", "v186", "v187", "v188", "v189", "v190", "v191", "v192", "v193", "v194", "v195", "v196", "v197", "v198", "v199", "v200", "v201", "v202", "v203", "v204", "v205", "v206", "v207", "v208", "v209", "v210", "v211", "v212", "v213", "v214", "v215", "v216", "v217", "v218", "v219", "v220", "v221", "v222", "v223", "v224", "v225", "v226", "v227", "v228", "v229", "v230", "v231", "v232", "v233", "v234", "v235", "v236", "v237", "v238", "v239", "v240", "v241", "v242", "v243", "v244", "v245", "v246", "v247", "v248", "v249", "v250", "v251", "v252", "v253", "v254", "v255", "a0", "a1", "a2", "a3", "a4", "a5", "a6", "a7", "a8", "a9", "a10", "a11", "a12", "a13", "a14", "a15", "a16", "a17", "a18", "a19", "a20", "a21", "a22", "a23", "a24", "a25", "a26", "a27", "a28", "a29", "a30", "a31", "a32", "a33", "a34", "a35", "a36", "a37", "a38", "a39", "a40", "a41", "a42", "a43", "a44", "a45", "a46", "a47", "a48", "a49", "a50", "a51", "a52", "a53", "a54", "a55", "a56", "a57", "a58", "a59", "a60", "a61", "a62", "a63", "a64", "a65", "a66", "a67", "a68", "a69", "a70", "a71", "a72", "a73", "a74", "a75", "a76", "a77", "a78", "a79", "a80", "a81", "a82", "a83", "a84", "a85", "a86", "a87", "a88", "a89", "a90", "a91", "a92", "a93", "a94", "a95", "a96", "a97", "a98", "a99", "a100", "a101", "a102", "a103", "a104", "a105", "a106", "a107", "a108", "a109", "a110", "a111", "a112", "a113", "a114", "a115", "a116", "a117", "a118", "a119", "a120", "a121", "a122", "a123", "a124", "a125", "a126", "a127", "a128", "a129", "a130", "a131", "a132", "a133", "a134", "a135", "a136", "a137", "a138", "a139", "a140", "a141", "a142", "a143", "a144", "a145", "a146", "a147", "a148", "a149", "a150", "a151", "a152", "a153", "a154", "a155", "a156", "a157", "a158", "a159", "a160", "a161", "a162", "a163", "a164", "a165", "a166", "a167", "a168", "a169", "a170", "a171", "a172", "a173", "a174", "a175", "a176", "a177", "a178", "a179", "a180", "a181", "a182", "a183", "a184", "a185", "a186", "a187", "a188", "a189", "a190", "a191", "s24", "s25", "s26", "s27", "s28", "s29", "s30", "s31", "s33", "s34", "s35", "s36", "s37", "s38", "s39", "s40", "s41", "s42", "s43", "s44", "s45", "s46", "s47", "s48", "s49", "s50", "s51", "s52", "s53", "s54", "s55", "s56", "s57", "s58", "s59", "s60", "s61", "s62", "s63", "s64", "s65", "s66", "s67", "s68", "s69", "s70", "s71", "s72", "s73", "s74", "s75", "s76", "s77", "s78", "s79", "s80", "s81", "s82", "s83", "s84", "s85", "s86", "s87", "s88", "s89", "s90", "s91", "s92", "s93", "s94", "s95", "s96", "s97", "s98", "s99", "vcc", "scc", "memory");
}

extern "C" void kernel_launch(void* const* d_in, const int* in_sizes, int n_in, void* d_out, int out_size, void* d_ws, size_t ws_size,
                              hipStream_t stream) {
  const unsigned kv_bytes = (unsigned)in_sizes[1] * 2u;
  paged_attn_kernel<<<256, 256, 0, stream>>>(d_in[0], d_in[1], d_in[2], (const int*)d_in[3], (const int*)d_in[4], d_out, kv_bytes);
}
```
